```python
import torch

def _softmax(x, axis=-1):
    return torch.softmax(x, dim=axis)
def _gelu(x):
    return torch.nn.functional.gelu(x, approximate="tanh")
def _silu(x):
    return torch.nn.functional.silu(x)
def _sigmoid(x):
    return torch.sigmoid(x)
def _layernorm(x, axis=-1, eps=1e-5):
    m = x.mean(dim=axis, keepdim=True)
    v = x.var(dim=axis, keepdim=True, unbiased=False)
    return (x - m) / torch.sqrt(v + eps)
def _scatter_add(tgt, idx, src, axis=0):
    return tgt.clone().index_add_(axis, idx, src)
def _segment_sum(src, seg, n):
    out = torch.zeros((n,) + src.shape[1:], dtype=src.dtype)
    return out.index_add_(0, seg, src)
def _pad(x, pw):
    flat = tuple(v for pair in reversed(pw) for v in pair)
    return torch.nn.functional.pad(x, flat)

kernel_name = 'recurrent_scan__mha_block__glu_gated_swiglu__prompt'

def setup_inputs():

    _w = (lambda a: (lambda t: t.to(torch.bfloat16) if t.is_floating_point() else t)(torch.as_tensor(a)))
    def _A_setup():
        B, S, D, H = 1, 2048, 512, 32
        return {
            "x": _w((lambda *s: torch.randn(*s, dtype=torch.bfloat16) * 0.05)(B, S, D)),
            "wqkv": _w((lambda *s: torch.randn(*s, dtype=torch.bfloat16) * 0.2)(D, 3 * D)),
            "wo": _w((lambda *s: torch.randn(*s, dtype=torch.bfloat16) * 0.2)(D, D)),
        }
    _A_in = _A_setup()

    def _B_setup():
        B, S, D, F = 1, 2048, 512, 1376
        return {
            "x": _w((lambda *s: torch.randn(*s, dtype=torch.bfloat16) * 0.05)(B, S, D)),
            "w1": _w((lambda *s: torch.randn(*s, dtype=torch.bfloat16) * 0.1)(D, F) * (2.0 / D) ** 0.5),
            "w3": _w((lambda *s: torch.randn(*s, dtype=torch.bfloat16) * 0.1)(D, F) * (2.0 / D) ** 0.5),
            "w2": _w((lambda *s: torch.randn(*s, dtype=torch.bfloat16) * 0.1)(F, D) * (2.0 / F) ** 0.5),
        }
    _B_in = _B_setup()

    return {
        "A_x": _A_in["x"],
        "A_wqkv": _A_in["wqkv"],
        "A_wo": _A_in["wo"],
        "B_x": _B_in["x"],
        "B_w1": _B_in["w1"],
        "B_w3": _B_in["w3"],
        "B_w2": _B_in["w2"],
    }

def reference(A_x, A_wqkv, A_wo, B_x, B_w1, B_w3, B_w2):
    def _A_ref(x, wqkv, wo):
        B, S, D = x.shape
        H = 32
        Dh = D // H
        qkv = (lambda x, *d: x.permute(*d))((x @ wqkv).reshape(B, S, 3, H, Dh), 2, 0, 3, 1, 4)
        q, k, v = qkv[0], qkv[1], qkv[2]
        scores = torch.einsum("bhqd,bhkd->bhqk", q, k) * (Dh ** -0.5)
        p = _softmax(scores, axis=-1)
        o = torch.einsum("bhqk,bhkd->bhqd", p, v)
        return (lambda x, *d: x.permute(*d))(o, 0, 2, 1, 3).reshape(B, S, D) @ wo

    def _B_ref(x, w1, w3, w2):
        gate = torch.einsum("bsd,df->bsf", x, w1)
        up = torch.einsum("bsd,df->bsf", x, w3)
        h = _silu(gate) * up
        out = torch.einsum("bsf,fd->bsd", h, w2)
        return out

    x = A_x
    xn = _layernorm(x, axis=-1)
    s = _A_ref(xn, A_wqkv, A_wo)
    g = _silu(_B_ref(xn, B_w1, B_w3, B_w2))
    return x + s * g
```

```cpp
#include <hip/hip_runtime.h>
#include <hip/hip_bf16.h>
#include <stdint.h>
typedef unsigned short u16;
__device__ inline float bf2f(u16 v){ return __uint_as_float(((unsigned)v)<<16); }
__device__ inline u16 f2bf(float f){ __hip_bfloat16 b = __float2bfloat16(f); return *(u16*)&b; }
constexpr int S=2048, D=512, H=32, DH=16, F=1376;

__global__ void ln_kernel(const u16* x, u16* xn){
  int row = blockIdx.x; int t = threadIdx.x;
  float v[8]; float s=0;
  for(int i=0;i<8;i++){ v[i]=bf2f(x[row*D + t*8+i]); s+=v[i]; }
  for(int o=32;o>0;o>>=1) s+=__shfl_xor(s,o);
  float m = s/D; float q=0;
  for(int i=0;i<8;i++){ float d=v[i]-m; q+=d*d; }
  for(int o=32;o>0;o>>=1) q+=__shfl_xor(q,o);
  float r = 1.f/sqrtf(q/D+1e-5f);
  for(int i=0;i<8;i++) xn[row*D+t*8+i]=f2bf((v[i]-m)*r);
}
__global__ void gemm_naive(const u16* A, const u16* B, u16* C, int M,int N,int K){
  int n = blockIdx.x*64+threadIdx.x; int m0 = blockIdx.y*8;
  if(n>=N) return;
  float acc[8]={0,0,0,0,0,0,0,0};
  for(int k=0;k<K;k++){ float b=bf2f(B[(size_t)k*N+n]);
    #pragma unroll
    for(int i=0;i<8;i++) acc[i]+=bf2f(A[(size_t)(m0+i)*K+k])*b; }
  for(int i=0;i<8;i++) C[(size_t)(m0+i)*N+n]=f2bf(acc[i]);
}
__global__ void attn_naive(const u16* qkv, u16* o){
  int h = blockIdx.y; int q = blockIdx.x*64+threadIdx.x;
  float qv[16]; for(int d=0;d<16;d++) qv[d]=bf2f(qkv[(size_t)q*1536+h*16+d])*0.25f;
  float m=-INFINITY,l=0,acc[16]; for(int d=0;d<16;d++) acc[d]=0;
  for(int k=0;k<S;k++){
    float s=0; for(int d=0;d<16;d++) s+=qv[d]*bf2f(qkv[(size_t)k*1536+512+h*16+d]);
    float mn=fmaxf(m,s); float a=__expf(m-mn); float p=__expf(s-mn);
    l=l*a+p;
    for(int d=0;d<16;d++) acc[d]=acc[d]*a+p*bf2f(qkv[(size_t)k*1536+1024+h*16+d]);
    m=mn;
  }
  for(int d=0;d<16;d++) o[(size_t)q*512+h*16+d]=f2bf(acc[d]/l);
}
__global__ void swiglu(const u16* g,const u16* u,u16* h,int n){ int i=blockIdx.x*256+threadIdx.x; if(i<n){ float a=bf2f(g[i]); float s=a/(1.f+__expf(-a)); h[i]=f2bf(s*bf2f(u[i])); } }
__global__ void fin(const u16* x,const u16* s,const u16* f,u16* out,int n){ int i=blockIdx.x*256+threadIdx.x; if(i<n){ float a=bf2f(f[i]); float g=a/(1.f+__expf(-a)); out[i]=f2bf(bf2f(x[i])+bf2f(s[i])*g);} }

extern "C" void kernel_launch(void* const* d_in, const int* in_sizes, int n_in, void* d_out, int out_size, void* d_ws, size_t ws_size, hipStream_t stream){
  const u16* x=(const u16*)d_in[0]; const u16* wqkv=(const u16*)d_in[1]; const u16* wo=(const u16*)d_in[2];
  const u16* w1=(const u16*)d_in[4]; const u16* w3=(const u16*)d_in[5]; const u16* w2=(const u16*)d_in[6];
  u16* ws=(u16*)d_ws;
  u16* xn=ws; u16* qkv=xn+S*D; u16* o=qkv+S*1536; u16* s=o+S*D; u16* g=s+S*D; u16* u=g+S*F; u16* h=u+S*F; u16* f=h+S*F;
  ln_kernel<<<S,64,0,stream>>>(x,xn);
  gemm_naive<<<dim3(1536/64,S/8),64,0,stream>>>(xn,wqkv,qkv,S,1536,D);
  attn_naive<<<dim3(S/64,H),64,0,stream>>>(qkv,o);
  gemm_naive<<<dim3(512/64,S/8),64,0,stream>>>(o,wo,s,S,512,D);
  gemm_naive<<<dim3((F+63)/64,S/8),64,0,stream>>>(xn,w1,g,S,F,D);
  gemm_naive<<<dim3((F+63)/64,S/8),64,0,stream>>>(xn,w3,u,S,F,D);
  swiglu<<<(S*F+255)/256,256,0,stream>>>(g,u,h,S*F);
  gemm_naive<<<dim3(512/64,S/8),64,0,stream>>>(h,w2,f,S,512,F);
  fin<<<(S*D+255)/256,256,0,stream>>>(x,s,f,(u16*)d_out,S*D);
}
```

```cpp
#include <hip/hip_runtime.h>
#include <hip/hip_bf16.h>
#include <stdint.h>

typedef unsigned short u16;
typedef short s16x4 __attribute__((ext_vector_type(4)));
typedef short bf16x8 __attribute__((ext_vector_type(8)));
typedef float f32x16 __attribute__((ext_vector_type(16)));
typedef float f32x4 __attribute__((ext_vector_type(4)));
typedef unsigned u32x4 __attribute__((ext_vector_type(4)));
typedef unsigned u32x2 __attribute__((ext_vector_type(2)));
typedef __bf16 bf16x2 __attribute__((ext_vector_type(2)));
#define LAS __attribute__((address_space(3)))
typedef LAS const char* lds_cptr;
typedef LAS char* lds_ptr;

constexpr int SEQ = 2048, DM = 512, NH = 32, FF = 1376;
constexpr int NTT = 64;
constexpr int NFG = 86;
constexpr size_t WS_Q = 0, WS_K = 2u << 20, WS_V = 4u << 20, WS_XN = 6u << 20, WS_O = 8u << 20, WS_G = 10u << 20, WS_H = 12u << 20;
constexpr unsigned IMG_BYTES = 2u << 20, H_BYTES = NTT * NFG * 1024;

#define MFMA(a, b, c) __builtin_amdgcn_mfma_f32_32x32x16_bf16(a, b, c, 0, 0, 0)
#define SRD(p, bytes) __builtin_amdgcn_make_buffer_rsrc((void*)(p), (short)0, (int)(bytes), 0x00020000)
typedef __amdgpu_buffer_rsrc_t srd_t;

__device__ __forceinline__ void dma16(unsigned lds_base, unsigned voff, srd_t srd, unsigned soff) {
  asm volatile("s_mov_b32 m0, %0\n\ts_nop 0\n\tbuffer_load_dwordx4 %1, %2, %3 offen lds" ::"s"(lds_base), "v"(voff), "s"(srd), "s"(soff) : "memory");
}
__device__ __forceinline__ s16x4 tr_read(lds_cptr p) {
  return __builtin_bit_cast(s16x4, __builtin_amdgcn_ds_read_tr16_b64_v4i16((LAS s16x4*)p));
}
__device__ __forceinline__ bf16x8 tr_frag(lds_cptr p, int second) {
  s16x4 lo = tr_read(p), hi = tr_read(p + second);
  return (bf16x8){lo[0], lo[1], lo[2], lo[3], hi[0], hi[1], hi[2], hi[3]};
}
__device__ __forceinline__ unsigned cvtpk(float lo, float hi) {
  bf16x2 r = {(__bf16)lo, (__bf16)hi};
  return __builtin_bit_cast(unsigned, r);
}
__device__ __forceinline__ float dot2(unsigned a, unsigned b, float c) {
  return __builtin_amdgcn_fdot2_f32_bf16(__builtin_bit_cast(bf16x2, a), __builtin_bit_cast(bf16x2, b), c, false);
}
__device__ __forceinline__ float bflo(unsigned v) { return __uint_as_float(v << 16); }
__device__ __forceinline__ float bfhi(unsigned v) { return __uint_as_float(v & 0xffff0000u); }
__device__ __forceinline__ float half_sum(float v) {
  auto r = __builtin_amdgcn_permlane32_swap(__float_as_uint(v), __float_as_uint(v), false, false);
  return __uint_as_float(r[0]) + __uint_as_float(r[1]);
}
__device__ __forceinline__ float half_max(float v) {
  auto r = __builtin_amdgcn_permlane32_swap(__float_as_uint(v), __float_as_uint(v), false, false);
  return __builtin_fmaxf(__uint_as_float(r[0]), __uint_as_float(r[1]));
}
__device__ __forceinline__ u32x4 pack8(const f32x16& v, int b) {
  return (u32x4){cvtpk(v[b], v[b + 1]), cvtpk(v[b + 2], v[b + 3]), cvtpk(v[b + 4], v[b + 5]), cvtpk(v[b + 6], v[b + 7])};
}
#define WAITV(n) asm volatile("s_waitcnt vmcnt(%0)" ::"i"(n) : "memory")
#define WAITV_BAR(n) asm volatile("s_waitcnt vmcnt(%0) lgkmcnt(0)\n\ts_barrier" ::"i"(n) : "memory")

constexpr int K1_W = 0, K1_X = 98304, K1_LDS = 163840;
__global__ __launch_bounds__(256) void k_qkv(const u16* __restrict__ x, const u16* __restrict__ wqkv, char* __restrict__ ws) {
  extern __shared__ __attribute__((aligned(16))) char lds[];
  const int tid = threadIdx.x, lane = tid & 63;
  const int w = __builtin_amdgcn_readfirstlane(tid >> 6);
  const int xcd = blockIdx.x & 7, bi = blockIdx.x >> 3;
  const int panel = 4 * (xcd & 3) + (bi & 3), g = 8 * (xcd >> 2) + (bi >> 2);
  const int tt = 4 * panel + w;
  const unsigned lds0 = (unsigned)(uintptr_t)lds;
  const int h = lane >> 5, tok = lane & 31;
  const srd_t sx = SRD(x, SEQ * DM * 2), sw = SRD(wqkv, DM * 3 * DM * 2);
  const u32x4* xrow = (const u32x4*)(x + (size_t)(tt * 32 + tok) * DM + g * 32 + h * 8);
  const u32x4 xs0 = xrow[0], xs1 = xrow[2];
  const unsigned vW = (lane >> 2) * 3072 + (lane & 3) * 16;
  const unsigned vXa = (lane >> 3) * 1024 + (((lane & 7) ^ (lane >> 4)) * 16);
  const unsigned vXb = (lane >> 3) * 1024 + (((lane & 7) ^ (4 + (lane >> 4))) * 16);
  auto issueW = [&](int s) {
#pragma unroll
    for (int i = 0; i < 3; ++i) {
      const int idx = 3 * w + i, tile = idx >> 2, part = idx & 3;
      dma16(lds0 + K1_W + tile * 32768 + (64 * s + 16 * part) * 64, vW, sw, (64 * s + 16 * part) * 3072 + tile * 1024 + g * 64);
    }
  };
  auto issueX = [&](int s) {
#pragma unroll
    for (int i = 0; i < 4; ++i)
      dma16(lds0 + K1_X + (s & 3) * 16384 + (32 * w + 8 * i) * 128, (i & 1) ? vXb : vXa, sx, (tt * 32 + 8 * i) * 1024 + s * 128);
  };
  asm volatile("s_nop 4");
#pragma unroll
  for (int s = 0; s < 4; ++s) { issueW(s); issueX(s); }
#pragma unroll
  for (int s = 4; s < 8; ++s) issueW(s);

  const lds_cptr L = (lds_cptr)lds;
  const int grp1 = (lane >> 4) & 1, q = (lane & 15) >> 2, p = lane & 3;
  const lds_cptr wb = L + K1_W + (8 * h + q) * 64 + grp1 * 32 + p * 8;
  const lds_cptr wcs = wb + w * 1024;
  lds_cptr xb[4];
#pragma unroll
  for (int ks = 0; ks < 4; ++ks) xb[ks] = L + K1_X + (32 * w + tok) * 128 + (((2 * ks + h) ^ ((tok >> 1) & 7)) * 16);
  f32x16 aq = {}, ak = {}, av = {};
  float sx1 = 0.f, sx2 = 0.f, cq = 0.f, ck = 0.f, cv = 0.f;
  const unsigned ONE2 = 0x3f803f80u;
#pragma unroll
  for (int s = 0; s < 8; ++s) {
    if (s == 0) WAITV_BAR(33); else if (s == 1) WAITV_BAR(30); else if (s == 2) WAITV_BAR(27); else if (s == 3) WAITV_BAR(24);
    else if (s == 4) WAITV_BAR(12); else if (s == 5) WAITV_BAR(8); else if (s == 6) WAITV_BAR(4); else WAITV_BAR(0);
    bf16x8 xf[4];
#pragma unroll
    for (int ks = 0; ks < 4; ++ks) xf[ks] = *(LAS const bf16x8*)(xb[ks] + (s & 3) * 16384);
#pragma unroll
    for (int ks = 0; ks < 4; ++ks) {
      const int kk = 4 * s + ks;
      const bf16x8 wq = tr_frag(wb + kk * 1024, 256), wk = tr_frag(wb + 32768 + kk * 1024, 256), wv = tr_frag(wb + 65536 + kk * 1024, 256);
      aq = MFMA(wq, xf[ks], aq);
      ak = MFMA(wk, xf[ks], ak);
      av = MFMA(xf[ks], wv, av);
      const u32x4 xu = __builtin_bit_cast(u32x4, xf[ks]);
#pragma unroll
      for (int r = 0; r < 4; ++r) { sx1 = dot2(xu[r], ONE2, sx1); sx2 = dot2(xu[r], xu[r], sx2); }
    }
    {
      const u32x4 a = __builtin_bit_cast(u32x4, tr_frag(wcs + s * 4096, 256)), b = __builtin_bit_cast(u32x4, tr_frag(wcs + 32768 + s * 4096, 256)),
                  c = __builtin_bit_cast(u32x4, tr_frag(wcs + 65536 + s * 4096, 256));
#pragma unroll
      for (int r = 0; r < 4; ++r) { cq = dot2(a[r], ONE2, cq); ck = dot2(b[r], ONE2, ck); cv = dot2(c[r], ONE2, cv); }
    }
    if (s < 4) { asm volatile("s_waitcnt lgkmcnt(0)" ::: "memory"); issueX(s + 4); }
  }
  const float mean = half_sum(sx1) * (1.0f / DM);
  const float var = __builtin_fmaxf(half_sum(sx2) * (1.0f / DM) - mean * mean, 0.f);
  const float rstd = 1.0f / __builtin_sqrtf(var + 1e-5f);
  cq = half_sum(cq); ck = half_sum(ck); cv = half_sum(cv);
  LAS float* T = (LAS float*)(lds + K1_X);
  LAS float* csp = T;
  LAS float* cs = T + 384 + w * 96;
  LAS float* st = T + 768 + w * 64;
  if (lane < 32) { csp[w * 96 + tok] = cq; csp[w * 96 + 32 + tok] = ck; csp[w * 96 + 64 + tok] = cv; st[tok] = rstd; st[32 + tok] = -mean * rstd; }
  asm volatile("s_waitcnt lgkmcnt(0)\n\ts_barrier" ::: "memory");
  {
    const float a = (csp[lane] + csp[96 + lane]) + (csp[192 + lane] + csp[288 + lane]);
    cs[lane] = a;
    cv = (csp[64 + tok] + csp[96 + 64 + tok]) + (csp[192 + 64 + tok] + csp[288 + 64 + tok]);
  }
  asm volatile("s_waitcnt lgkmcnt(0)" ::: "memory");
  const float QS = 0.25f * 1.4426950408889634f;
  char* Qi = ws + WS_Q; char* Ki = ws + WS_K; char* Vi = ws + WS_V; char* Xi = ws + WS_XN;
  {
    f32x16 o;
#pragma unroll
    for (int rg = 0; rg < 4; ++rg) { const f32x4 c4 = *(LAS const f32x4*)(cs + 8 * rg + 4 * h);
#pragma unroll
      for (int i = 0; i < 4; ++i) o[4 * rg + i] = (aq[4 * rg + i] - mean * c4[i]) * (rstd * QS); }
    *(u32x4*)(Qi + ((2 * g) * 64 + tt) * 1024 + lane * 16) = pack8(o, 0);
    *(u32x4*)(Qi + ((2 * g + 1) * 64 + tt) * 1024 + lane * 16) = pack8(o, 8);
#pragma unroll
    for (int rg = 0; rg < 4; ++rg) { const f32x4 c4 = *(LAS const f32x4*)(cs + 32 + 8 * rg + 4 * h);
#pragma unroll
      for (int i = 0; i < 4; ++i) o[4 * rg + i] = (ak[4 * rg + i] - mean * c4[i]) * rstd; }
    *(u32x4*)(Ki + ((2 * g) * 64 + tt) * 1024 + lane * 16) = pack8(o, 0);
    *(u32x4*)(Ki + ((2 * g + 1) * 64 + tt) * 1024 + lane * 16) = pack8(o, 8);
#pragma unroll
    for (int rg = 0; rg < 4; ++rg) { const f32x4 r4 = *(LAS const f32x4*)(st + 8 * rg + 4 * h), m4 = *(LAS const f32x4*)(st + 32 + 8 * rg + 4 * h);
#pragma unroll
      for (int i = 0; i < 4; ++i) o[4 * rg + i] = av[4 * rg + i] * r4[i] + m4[i] * cv; }
    char* vp = Vi + ((2 * g + grp1) * 64 + tt) * 1024 + h * 256 + (lane & 15) * 16;
    *(u32x4*)(vp) = pack8(o, 0);
    *(u32x4*)(vp + 512) = pack8(o, 8);
    const float nm = -mean * rstd;
    u32x4 y;
#pragma unroll
    for (int r = 0; r < 4; ++r) y[r] = cvtpk(bflo(xs0[r]) * rstd + nm, bfhi(xs0[r]) * rstd + nm);
    *(u32x4*)(Xi + (tt * 32 + 2 * g) * 1024 + lane * 16) = y;
#pragma unroll
    for (int r = 0; r < 4; ++r) y[r] = cvtpk(bflo(xs1[r]) * rstd + nm, bfhi(xs1[r]) * rstd + nm);
    *(u32x4*)(Xi + (tt * 32 + 2 * g + 1) * 1024 + lane * 16) = y;
  }
}

constexpr float REF_OFF = 88.f;
#define ATT_UNROLL 8
#define STR2(x) #x
#define STR(x) STR2(x)
#define ATT_CLOB "v64","v65","v66","v67","v68","v69","v70","v71","v72","v73","v74","v75","v76","v77","v78","v79", \
  "v80","v81","v82","v83","v84","v85","v86","v87","v88","v89","v90","v91","v92","v93","v94","v95", \
  "v96","v97","v98","v99","v100","v101","v102","v103","v104","v105","v106","v107", \
  "v108","v109","v110","v111","v112","v113","v114","v115","v116","v117","v118","v119","v120","v121","v122","v123"
__device__ __forceinline__ void attn_fast(f32x16& o, const f32x16& c, const bf16x8& qf, unsigned kaddr, unsigned vaddr) {
  const unsigned long long vmask = 0x0000ffff0000ffffull;
  asm volatile(
      ".macro ATT_TILE X, Y, VC, VN, T\n"
      "s_waitcnt lgkmcnt(0)\n"
      "v_cvt_pk_bf16_f32 v103, v[\\Y+14], v[\\Y+15]\n"
      "v_mfma_f32_32x32x16_bf16 v[\\Y:\\Y+15], v[104:107], %[q], %[c]\n"
      "ds_read_b128 v[104:107], %[ka] offset:(\\T+2)*1024\n"
      "v_exp_f32 v[\\X+0], v[\\X+0]\n"
      "v_exp_f32 v[\\X+1], v[\\X+1]\n"
      "v_exp_f32 v[\\X+2], v[\\X+2]\n"
      "v_cvt_pk_bf16_f32 v96, v[\\X+0], v[\\X+1]\n"
      "v_exp_f32 v[\\X+3], v[\\X+3]\n"
      "v_mfma_f32_32x32x16_bf16 %[o], v[\\VN+4:\\VN+7], v[100:103], %[o]\n"
      "s_mov_b64 exec, %[m]\n"
      "ds_read_b128 v[\\VN:\\VN+3], %[va] offset:(\\T+1)*1024\n"
      "ds_read_b128 v[\\VN+4:\\VN+7], %[va] offset:(\\T+1)*1024+512\n"
      "s_mov_b64 exec, -1\n"
      "v_exp_f32 v[\\X+4], v[\\X+4]\n"
      "v_cvt_pk_bf16_f32 v97, v[\\X+2], v[\\X+3]\n"
      "v_exp_f32 v[\\X+5], v[\\X+5]\n"
      "v_exp_f32 v[\\X+6], v[\\X+6]\n"
      "v_cvt_pk_bf16_f32 v98, v[\\X+4], v[\\X+5]\n"
      "v_exp_f32 v[\\X+7], v[\\X+7]\n"
      "v_exp_f32 v[\\X+8], v[\\X+8]\n"
      "v_cvt_pk_bf16_f32 v99, v[\\X+6], v[\\X+7]\n"
      "v_exp_f32 v[\\X+9], v[\\X+9]\n"
      "v_exp_f32 v[\\X+10], v[\\X+10]\n"
      "v_mfma_f32_32x32x16_bf16 %[o], v[\\VC:\\VC+3], v[96:99], %[o]\n"
      "v_cvt_pk_bf16_f32 v100, v[\\X+8], v[\\X+9]\n"
      "v_exp_f32 v[\\X+11], v[\\X+11]\n"
      "v_exp_f32 v[\\X+12], v[\\X+12]\n"
      "v_cvt_pk_bf16_f32 v101, v[\\X+10], v[\\X+11]\n"
      "v_exp_f32 v[\\X+13], v[\\X+13]\n"
      "v_exp_f32 v[\\X+14], v[\\X+14]\n"
      "v_cvt_pk_bf16_f32 v102, v[\\X+12], v[\\X+13]\n"
      "v_exp_f32 v[\\X+15], v[\\X+15]\n"
      ".endm\n"
      ".set att_i, 108\n"
      ".rept 16\n"
      "v_mov_b32 v[att_i], 0x3f803f80\n"
      ".set att_i, att_i+1\n"
      ".endr\n"
      "v_mov_b32 v100, 0\n v_mov_b32 v101, 0\n v_mov_b32 v102, 0\n v_mov_b32 v94, 0\n v_mov_b32 v95, 0\n"
      "ds_read_b128 v[104:107], %[ka]\n"
      "s_mov_b64 exec, %[m]\n"
      "ds_read_b128 v[108:111], %[va]\n"
      "ds_read_b128 v[112:115], %[va] offset:512\n"
      "s_mov_b64 exec, -1\n"
      "s_waitcnt lgkmcnt(2)\n"
      "v_mfma_f32_32x32x16_bf16 v[64:79], v[104:107], %[q], %[c]\n"
      "ds_read_b128 v[104:107], %[ka] offset:1024\n"
      "s_nop 7\n s_nop 7\n"
      "s_movk_i32 s40, " STR(64 / ATT_UNROLL) "\n"
      "att_loop%=:\n"
      ".set att_t, 0\n"
      ".rept " STR(ATT_UNROLL / 2) "\n"
      "ATT_TILE 64, 80, 108, 116, att_t\n"
      "ATT_TILE 80, 64, 116, 108, att_t+1\n"
      ".set att_t, att_t+2\n"
      ".endr\n"
      "v_add_u32 %[ka], " STR(ATT_UNROLL * 1024) ", %[ka]\n"
      "v_add_u32 %[va], " STR(ATT_UNROLL * 1024) ", %[va]\n"
      "s_sub_u32 s40, s40, 1\n"
      "s_cmp_lg_u32 s40, 0\n"
      "s_cbranch_scc1 att_loop%=\n"
      "s_waitcnt lgkmcnt(0)\n"
      "v_cvt_pk_bf16_f32 v103, v94, v95\n"
      "s_nop 1\n"
      "v_mfma_f32_32x32x16_bf16 %[o], v[120:123], v[100:103], %[o]\n"
      "s_nop 7\n s_nop 7\n"
      ".purgem ATT_TILE\n"
      : [o] "+v"(o), [ka] "+v"(kaddr), [va] "+v"(vaddr)
      : [c] "v"(c), [q] "v"(qf), [m] "s"(vmask)
      : "memory", "s40", "scc", ATT_CLOB);
}

__device__ __forceinline__ void attn_safe(f32x16& o, bf16x8 qf, lds_cptr kp, lds_cptr vp, int lane) {
  o = f32x16{};
  float m = -INFINITY;
  const bf16x8 ones = {0x3f80, 0x3f80, 0x3f80, 0x3f80, 0x3f80, 0x3f80, 0x3f80, 0x3f80};
#pragma unroll 1
  for (int t = 0; t < NTT; ++t) {
    const bf16x8 kf = *(LAS const bf16x8*)(kp + t * 1024);
    f32x16 s = MFMA(kf, qf, f32x16{});
    float mx = s[0];
#pragma unroll
    for (int r = 1; r < 16; ++r) mx = __builtin_fmaxf(mx, s[r]);
    mx = __builtin_fmaxf(half_max(mx), m);
    const float a = __builtin_amdgcn_exp2f(m - mx);
    m = mx;
#pragma unroll
    for (int r = 0; r < 16; ++r) { o[r] *= a; s[r] = __builtin_amdgcn_exp2f(s[r] - m); }
    bf16x8 v0 = *(LAS const bf16x8*)(vp + t * 1024), v1 = *(LAS const bf16x8*)(vp + t * 1024 + 512);
    if (lane & 16) { v0 = ones; v1 = ones; }
    o = MFMA(v0, __builtin_bit_cast(bf16x8, pack8(s, 0)), o);
    o = MFMA(v1, __builtin_bit_cast(bf16x8, pack8(s, 8)), o);
  }
}

__device__ __forceinline__ void attn_wave(const char* lds, bf16x8 qf, char* optr, int lane) {
  const lds_cptr kp = (lds_cptr)lds + lane * 16;
  const lds_cptr vp = (lds_cptr)lds + 65536 + (lane >> 5) * 256 + (lane & 15) * 16;
  float mx;
  {
    const f32x16 s0 = MFMA(*(LAS const bf16x8*)(kp), qf, f32x16{}), s1 = MFMA(*(LAS const bf16x8*)(kp + 1024), qf, f32x16{});
    mx = __builtin_fmaxf(s0[0], s1[0]);
#pragma unroll
    for (int r = 1; r < 16; ++r) mx = __builtin_fmaxf(__builtin_fmaxf(mx, s0[r]), s1[r]);
    mx = half_max(mx);
  }
  const float nref = -(mx + REF_OFF);
  f32x16 c, o = {};
#pragma unroll
  for (int r = 0; r < 16; ++r) c[r] = nref;
  attn_fast(o, c, qf, (unsigned)(uintptr_t)kp, (unsigned)(uintptr_t)vp);
  float amax = 0.f; bool bad = !(o[8] < 0x1p126f);
#pragma unroll
  for (int r = 0; r < 8; ++r) { const float a = __builtin_fabsf(o[r]); bad |= !(a < 0x1p126f); amax = __builtin_fmaxf(amax, a); }
  bad |= !(amax > 0x1p-100f);
  if (__builtin_expect(__any(bad), 0)) attn_safe(o, qf, kp, vp, lane);
  const float rl = 1.0f / o[8];
#pragma unroll
  for (int r = 0; r < 8; ++r) o[r] *= rl;
  *(u32x4*)(optr + lane * 16) = pack8(o, 0);
}

__global__ __launch_bounds__(512) void k_attn(char* __restrict__ ws) {
  extern __shared__ __attribute__((aligned(16))) char lds[];
  const int tid = threadIdx.x, lane = tid & 63;
  const int w = __builtin_amdgcn_readfirstlane(tid >> 6);
  const int xcd = blockIdx.x & 7, bi = blockIdx.x >> 3;
  const int hd = 4 * xcd + (bi >> 3), qb = bi & 7, tt = qb * 8 + w;
  const unsigned lds0 = (unsigned)(uintptr_t)lds;
  const srd_t sk = SRD(ws + WS_K, IMG_BYTES), sv = SRD(ws + WS_V, IMG_BYTES);
  const bf16x8 qf = *(const bf16x8*)(ws + WS_Q + (hd * 64 + tt) * 1024 + lane * 16);
  asm volatile("s_nop 4");
#pragma unroll
  for (int c = 0; c < 8; ++c) {
    dma16(lds0 + (c * 8 + w) * 1024, lane * 16, sk, hd * 65536 + (c * 8 + w) * 1024);
    dma16(lds0 + 65536 + (c * 8 + w) * 1024, lane * 16, sv, hd * 65536 + (c * 8 + w) * 1024);
  }
  WAITV_BAR(0);
  attn_wave(lds, qf, ws + WS_O + (tt * 32 + hd) * 1024, lane);
}

#define FBAR(n) WAITV_BAR(n)
__device__ __forceinline__ float silu(float v) { return v * __builtin_amdgcn_rcpf(1.0f + __builtin_amdgcn_exp2f(-1.4426950408889634f * v)); }

__device__ __forceinline__ void ffn_up(char* lds, const u16* w1, const u16* w3, char* ws, int panel, int g, int w, int lane) {
  const unsigned lds0 = (unsigned)(uintptr_t)lds;
  const int tt = 4 * panel + w, h = lane >> 5;
  const int npairs = g < 11 ? 3 : 2, pr0 = g < 11 ? 3 * g : 33 + 2 * (g - 11);
  const srd_t s1 = SRD(w1, DM * FF * 2), s3 = SRD(w3, DM * FF * 2);
  const unsigned vg = (lane >> 2) * (FF * 2) + (lane & 3) * 16, vu = (lane >> 2) * (FF * 2) + ((lane & 3) ^ 2) * 16;
  auto issue = [&](int j, int slot) {
    const unsigned so = (64 * (j & 7) + 16 * w) * (FF * 2) + (pr0 + (j >> 3)) * 64;
    dma16(lds0 + slot * 8192 + w * 1024, vg, s1, so);
    dma16(lds0 + slot * 8192 + 4096 + w * 1024, vu, s3, so);
  };
  asm volatile("s_nop 4");
  issue(0, 0); issue(1, 1);
  bf16x8 xf[32];
#pragma unroll
  for (int kk = 0; kk < 32; ++kk) xf[kk] = *(const bf16x8*)(ws + WS_XN + (tt * 32 + kk) * 1024 + lane * 16);
  const int grp1 = (lane >> 4) & 1, q = (lane & 15) >> 2, p = lane & 3;
  const lds_cptr L = (lds_cptr)lds;
  const lds_cptr b0 = L + (grp1 ? 4096 + 32 : 0) + (8 * h + q) * 64 + p * 8, b1 = L + (grp1 ? 4096 : 32) + (8 * h + q) * 64 + p * 8;
  const int nst = npairs * 8;
  int slot = 0;
  asm volatile("s_waitcnt vmcnt(0)" ::: "memory");
  for (int pi = 0; pi < npairs; ++pi) {
    f32x16 a0 = {}, a1 = {};
#pragma unroll
    for (int st = 0; st < 8; ++st) {
      const int j = pi * 8 + st;
      FBAR(2);
      { const int jn = j + 2 < nst ? j + 2 : j; int sn = slot + 2; sn = sn >= 3 ? sn - 3 : sn; issue(jn, sn); }
      const lds_cptr c0 = b0 + slot * 8192, c1 = b1 + slot * 8192;
#pragma unroll
      for (int ks = 0; ks < 4; ++ks) {
        a0 = MFMA(tr_frag(c0 + ks * 1024, 256), xf[4 * st + ks], a0);
        a1 = MFMA(tr_frag(c1 + ks * 1024, 256), xf[4 * st + ks], a1);
      }
      slot = slot == 2 ? 0 : slot + 1;
    }
    f32x16 hv;
#pragma unroll
    for (int r = 0; r < 8; ++r) { hv[r] = silu(a0[r]) * a0[8 + r]; hv[8 + r] = silu(a1[r]) * a1[8 + r]; }
    char* hp = ws + WS_H + (tt * NFG + 2 * (pr0 + pi)) * 1024 + lane * 16;
    *(u32x4*)(hp) = pack8(hv, 0);
    *(u32x4*)(hp + 1024) = pack8(hv, 8);
  }
  asm volatile("s_waitcnt vmcnt(0)" ::: "memory");
}

__global__ __launch_bounds__(256) void k_up(const u16* __restrict__ w1, const u16* __restrict__ w3, char* __restrict__ ws) {
  extern __shared__ __attribute__((aligned(16))) char lds[];
  const int lane = threadIdx.x & 63, w = __builtin_amdgcn_readfirstlane(threadIdx.x >> 6);
  const int xcd = blockIdx.x & 7, bi = blockIdx.x >> 3;
  ffn_up(lds, w1, w3, ws, 2 * xcd + (bi >> 4), bi & 15, w, lane);
}

constexpr int DN_NS = 3;
__device__ __forceinline__ void ffn_down(char* lds, const u16* w2, char* ws, int tb, int nb, int blk, int w, int lane) {
  const unsigned lds0 = (unsigned)(uintptr_t)lds + w * (DN_NS * 2048);
  const int h = lane >> 5;
  const int k0 = w < 2 ? 22 * w : 44 + 21 * (w - 2), nst = w < 2 ? 22 : 21;
  const srd_t s2 = SRD(w2, FF * DM * 2), sh = SRD(ws + WS_H, H_BYTES);
  const unsigned vw = (lane >> 3) * 1024 + (((lane & 7) ^ (((lane >> 4) & 1) << 2)) * 16), vh = lane * 16;
  const int grp1 = (lane >> 4) & 1, q = (lane & 15) >> 2, p = lane & 3;
  const lds_cptr L = (lds_cptr)lds + w * (DN_NS * 2048);
  const lds_cptr b0 = L + (4 * h + q) * 128 + ((q >> 1) & 1) * 64 + grp1 * 32 + p * 8;
  const lds_cptr b1 = L + (4 * h + q) * 128 + (((q >> 1) & 1) ^ 1) * 64 + grp1 * 32 + p * 8;
  u32x4 hf[DN_NS][2];
  auto issue = [&](int j) {
    const int kk = k0 + (j < nst ? j : 0);
    const unsigned so = kk * 16 * 1024 + nb * 128;
    dma16(lds0 + (j % DN_NS) * 2048, vw, s2, so);
    dma16(lds0 + (j % DN_NS) * 2048 + 1024, vw, s2, so + 8192);
    const unsigned ho = j < nst ? (2 * tb * NFG + kk) * 1024 : 0x7ff00000u;
    asm volatile("buffer_load_dwordx4 %0, %2, %3, %4 offen\n\tbuffer_load_dwordx4 %1, %2, %3, %5 offen"
                 : "=&v"(hf[j % DN_NS][0]), "=&v"(hf[j % DN_NS][1]) : "v"(vh), "s"(sh), "s"(ho), "s"(ho + (j < nst ? NFG * 1024 : 0)) : "memory");
  };
  asm volatile("s_nop 4");
#pragma unroll
  for (int j = 0; j < DN_NS - 1; ++j) issue(j);
  f32x16 a00 = {}, a01 = {}, a10 = {}, a11 = {};
#pragma unroll
  for (int j = 0; j < 22; ++j) {
    if (j + DN_NS - 2 < 22) asm volatile("s_waitcnt vmcnt(%2) lgkmcnt(0)" : "+v"(hf[j % DN_NS][0]), "+v"(hf[j % DN_NS][1]) : "i"(4 * (DN_NS - 2)) : "memory");
    else asm volatile("s_waitcnt vmcnt(0) lgkmcnt(0)" : "+v"(hf[j % DN_NS][0]), "+v"(hf[j % DN_NS][1])::"memory");
    const bf16x8 w0 = tr_frag(b0 + (j % DN_NS) * 2048, 1024), w1f = tr_frag(b1 + (j % DN_NS) * 2048, 1024);
    const bf16x8 h0 = __builtin_bit_cast(bf16x8, hf[j % DN_NS][0]), h1 = __builtin_bit_cast(bf16x8, hf[j % DN_NS][1]);
    if (j + DN_NS - 1 < 22) issue(j + DN_NS - 1);
    a00 = MFMA(w0, h0, a00); a01 = MFMA(w0, h1, a01);
    a10 = MFMA(w1f, h0, a10); a11 = MFMA(w1f, h1, a11);
  }
  LAS f32x4* R = (LAS f32x4*)lds;
  f32x16 mine = w == 0 ? a00 : w == 1 ? a01 : w == 2 ? a10 : a11;
#pragma unroll
  for (int r = 1; r < 4; ++r) {
    const int dst = (w + r) & 3;
    const f32x16 snd = dst == 0 ? a00 : dst == 1 ? a01 : dst == 2 ? a10 : a11;
    asm volatile("s_waitcnt lgkmcnt(0)\n\ts_barrier" ::: "memory");
#pragma unroll
    for (int i = 0; i < 4; ++i) R[(dst * 4 + i) * 64 + lane] = (f32x4){snd[4 * i], snd[4 * i + 1], snd[4 * i + 2], snd[4 * i + 3]};
    asm volatile("s_waitcnt lgkmcnt(0)\n\ts_barrier" ::: "memory");
#pragma unroll
    for (int i = 0; i < 4; ++i) { const f32x4 v = R[(w * 4 + i) * 64 + lane]; mine[4 * i] += v[0]; mine[4 * i + 1] += v[1]; mine[4 * i + 2] += v[2]; mine[4 * i + 3] += v[3]; }
  }
#pragma unroll
  for (int r = 0; r < 16; ++r) mine[r] = silu(mine[r]);
  char* gp = ws + WS_G + (blk * 4 + w) * 2048 + lane * 16;
  *(u32x4*)(gp) = pack8(mine, 0);
  *(u32x4*)(gp + 1024) = pack8(mine, 8);
}

__global__ __launch_bounds__(256) void k_down(const u16* __restrict__ w2, char* __restrict__ ws) {
  extern __shared__ __attribute__((aligned(16))) char lds[];
  const int lane = threadIdx.x & 63, w = __builtin_amdgcn_readfirstlane(threadIdx.x >> 6);
  const int xcd = blockIdx.x & 7, bi = blockIdx.x >> 3;
  const int panel = 2 * xcd + (bi >> 4), g = bi & 15;
  ffn_down(lds, w2, ws, 2 * panel + (g >> 3), g & 7, blockIdx.x, w, lane);
}

__global__ __launch_bounds__(256) void k_fin(const u16* __restrict__ x, const u16* __restrict__ wo, const char* __restrict__ ws, u16* __restrict__ out) {
  extern __shared__ __attribute__((aligned(16))) char lds[];
  const int lane = threadIdx.x & 63, w = __builtin_amdgcn_readfirstlane(threadIdx.x >> 6);
  const int xcd = blockIdx.x & 7, bi = blockIdx.x >> 3;
  const int panel = 2 * xcd + (bi >> 4), gg = bi & 15, tb = 2 * panel + (gg >> 3), nb = gg & 7;
  const int nh = w >> 1, th = w & 1, tt = 2 * tb + th, h = lane >> 5;
  const unsigned lds0 = (unsigned)(uintptr_t)lds;
  const srd_t so = SRD(wo, DM * DM * 2);
  const unsigned vw = (lane >> 3) * 1024 + (((lane & 7) ^ (((lane >> 4) & 1) << 2)) * 16);
  asm volatile("s_nop 4");
#pragma unroll
  for (int i = 0; i < 16; ++i) dma16(lds0 + (4 * i + w) * 1024, vw, so, (4 * i + w) * 8192 + nb * 128);
  bf16x8 of[32];
#pragma unroll
  for (int hd = 0; hd < 32; ++hd) of[hd] = *(const bf16x8*)(ws + WS_O + (tt * 32 + hd) * 1024 + lane * 16);
  const char* gp = ws + WS_G + (blockIdx.x * 4 + w) * 2048 + lane * 16;
  const u32x4 g0 = *(const u32x4*)gp, g1 = *(const u32x4*)(gp + 1024);
  const size_t ob = (size_t)(tt * 32 + (lane & 31)) * DM + nb * 64 + nh * 32 + 4 * h;
  u32x2 xv[4];
#pragma unroll
  for (int i = 0; i < 4; ++i) xv[i] = *(const u32x2*)(x + ob + 8 * i);
  WAITV_BAR(0);
  const int grp1 = (lane >> 4) & 1, q = (lane & 15) >> 2, p = lane & 3;
  const lds_cptr b = (lds_cptr)lds + (4 * h + q) * 128 + ((((q >> 1) & 1) ^ nh) * 64) + grp1 * 32 + p * 8;
  f32x16 a = {};
#pragma unroll
  for (int hd = 0; hd < 32; ++hd) a = MFMA(tr_frag(b + hd * 2048, 1024), of[hd], a);
#pragma unroll
  for (int i = 0; i < 4; ++i) {
    const unsigned ga = i < 2 ? g0[2 * i] : g1[2 * i - 4], gb = i < 2 ? g0[2 * i + 1] : g1[2 * i - 3];
    u32x2 y;
    y[0] = cvtpk(bflo(xv[i][0]) + a[4 * i] * bflo(ga), bfhi(xv[i][0]) + a[4 * i + 1] * bfhi(ga));
    y[1] = cvtpk(bflo(xv[i][1]) + a[4 * i + 2] * bflo(gb), bfhi(xv[i][1]) + a[4 * i + 3] * bfhi(gb));
    *(u32x2*)(out + ob + 8 * i) = y;
  }
}

extern "C" void kernel_launch(void* const* d_in, const int* in_sizes, int n_in, void* d_out, int out_size, void* d_ws, size_t ws_size, hipStream_t stream) {
  const u16* x = (const u16*)d_in[0]; const u16* wqkv = (const u16*)d_in[1]; const u16* wo = (const u16*)d_in[2];
  const u16* w1 = (const u16*)d_in[4]; const u16* w3 = (const u16*)d_in[5]; const u16* w2 = (const u16*)d_in[6];
  char* ws = (char*)d_ws;
  (void)hipFuncSetAttribute((const void*)k_qkv, hipFuncAttributeMaxDynamicSharedMemorySize, K1_LDS);
  (void)hipFuncSetAttribute((const void*)k_attn, hipFuncAttributeMaxDynamicSharedMemorySize, 132096);
  (void)hipFuncSetAttribute((const void*)k_fin, hipFuncAttributeMaxDynamicSharedMemorySize, 65536);
  k_qkv<<<256, 256, K1_LDS, stream>>>(x, wqkv, ws);
  k_attn<<<256, 512, 132096, stream>>>(ws);
  k_up<<<256, 256, 3 * 8192, stream>>>(w1, w3, ws);
  k_down<<<256, 256, 4 * DN_NS * 2048, stream>>>(w2, ws);
  k_fin<<<256, 256, 65536, stream>>>(x, wo, ws, (u16*)d_out);
}
```

```cpp
#include <hip/hip_runtime.h>
#include <hip/hip_bf16.h>
#include <stdint.h>

typedef unsigned short u16;
typedef short s16x4 __attribute__((ext_vector_type(4)));
typedef short bf16x8 __attribute__((ext_vector_type(8)));
typedef float f32x16 __attribute__((ext_vector_type(16)));
typedef float f32x4 __attribute__((ext_vector_type(4)));
typedef unsigned u32x4 __attribute__((ext_vector_type(4)));
typedef unsigned u32x2 __attribute__((ext_vector_type(2)));
typedef __bf16 bf16x2 __attribute__((ext_vector_type(2)));
#define LAS __attribute__((address_space(3)))
typedef LAS const char* lds_cptr;
typedef LAS char* lds_ptr;

constexpr int SEQ = 2048, DM = 512, NH = 32, FF = 1376;
constexpr int NTT = 64;
constexpr int NFG = 86;
constexpr size_t WS_Q = 0, WS_K = 2u << 20, WS_V = 4u << 20, WS_XN = 6u << 20, WS_O = 8u << 20, WS_G = 10u << 20, WS_H = 12u << 20;
constexpr size_t WS_SYNC = 20u << 20;
constexpr unsigned IMG_BYTES = 2u << 20, H_BYTES = NTT * NFG * 1024;

#define MFMA(a, b, c) __builtin_amdgcn_mfma_f32_32x32x16_bf16(a, b, c, 0, 0, 0)
#define SRD(p, bytes) __builtin_amdgcn_make_buffer_rsrc((void*)(p), (short)0, (int)(bytes), 0x00020000)
typedef __amdgpu_buffer_rsrc_t srd_t;

__device__ __forceinline__ void dma16(unsigned lds_base, unsigned voff, srd_t srd, unsigned soff) {
  asm volatile("s_mov_b32 m0, %0\n\ts_nop 0\n\tbuffer_load_dwordx4 %1, %2, %3 offen lds" ::"s"(lds_base), "v"(voff), "s"(srd), "s"(soff) : "memory");
}
__device__ __forceinline__ s16x4 tr_read(lds_cptr p) {
  return __builtin_bit_cast(s16x4, __builtin_amdgcn_ds_read_tr16_b64_v4i16((LAS s16x4*)p));
}
__device__ __forceinline__ bf16x8 tr_frag(lds_cptr p, int second) {
  s16x4 lo = tr_read(p), hi = tr_read(p + second);
  return (bf16x8){lo[0], lo[1], lo[2], lo[3], hi[0], hi[1], hi[2], hi[3]};
}
__device__ __forceinline__ unsigned cvtpk(float lo, float hi) {
  bf16x2 r = {(__bf16)lo, (__bf16)hi};
  return __builtin_bit_cast(unsigned, r);
}
__device__ __forceinline__ float dot2(unsigned a, unsigned b, float c) {
  return __builtin_amdgcn_fdot2_f32_bf16(__builtin_bit_cast(bf16x2, a), __builtin_bit_cast(bf16x2, b), c, false);
}
__device__ __forceinline__ float bflo(unsigned v) { return __uint_as_float(v << 16); }
__device__ __forceinline__ float bfhi(unsigned v) { return __uint_as_float(v & 0xffff0000u); }
__device__ __forceinline__ float half_sum(float v) {
  auto r = __builtin_amdgcn_permlane32_swap(__float_as_uint(v), __float_as_uint(v), false, false);
  return __uint_as_float(r[0]) + __uint_as_float(r[1]);
}
__device__ __forceinline__ float half_max(float v) {
  auto r = __builtin_amdgcn_permlane32_swap(__float_as_uint(v), __float_as_uint(v), false, false);
  return __builtin_fmaxf(__uint_as_float(r[0]), __uint_as_float(r[1]));
}
__device__ __forceinline__ u32x4 pack8(const f32x16& v, int b) {
  return (u32x4){cvtpk(v[b], v[b + 1]), cvtpk(v[b + 2], v[b + 3]), cvtpk(v[b + 4], v[b + 5]), cvtpk(v[b + 6], v[b + 7])};
}
#define SB() __builtin_amdgcn_sched_barrier(0)
#define WAITV(n) asm volatile("s_waitcnt vmcnt(%0)" ::"i"(n) : "memory")
#define WAITV_BAR(n) asm volatile("s_waitcnt vmcnt(%0) lgkmcnt(0)\n\ts_barrier" ::"i"(n) : "memory")

#define STAMP(k, i)
#define TR(i)
constexpr int K1_SLOT = 28672, K1_NS = 5, K1_TAB = K1_NS * K1_SLOT, K1_LDS = K1_TAB + 2048;
#define K1ROT ((bi & 3) * 2)
__global__ __launch_bounds__(768) void k_qkv(const u16* __restrict__ x, const u16* __restrict__ wqkv, char* __restrict__ ws) {
  extern __shared__ __attribute__((aligned(16))) char lds[];
  const int tid = threadIdx.x, lane = tid & 63;
  const int wid = __builtin_amdgcn_readfirstlane(tid >> 6), w = wid & 3;
  const int xcd = blockIdx.x & 7, bi = blockIdx.x >> 3;
  const int panel = 4 * (xcd & 3) + (bi & 3), g = 8 * (xcd >> 2) + (bi >> 2);
  const int tt = 4 * panel + w;
  const int h = lane >> 5, tok = lane & 31;
  const lds_cptr L = (lds_cptr)lds;
  const int grp1 = (lane >> 4) & 1, q = (lane & 15) >> 2, p = lane & 3;
  const lds_cptr wb = L + 16384 + (8 * h + q) * 64 + grp1 * 32 + p * 8;
  unsigned xb_[3][4], wb_[3];
#pragma unroll
  for (int sp = 0; sp < 3; ++sp) {
    wb_[sp] = (unsigned)(uintptr_t)wb + sp * 2 * K1_SLOT; asm volatile("" : "+v"(wb_[sp]));
#pragma unroll
    for (int ks = 0; ks < 4; ++ks) { xb_[sp][ks] = (unsigned)(uintptr_t)L + (32 * w + tok) * 128 + (((2 * ks + h) ^ ((tok >> 1) & 7)) * 16) + sp * 2 * K1_SLOT; asm volatile("" : "+v"(xb_[sp][ks])); }
  }
#define K1_XP(s, ks) ((lds_cptr)(uintptr_t)xb_[((s) % K1_NS) >> 1][ks] + (((s) % K1_NS) & 1) * K1_SLOT)
#define K1_WP(s) ((lds_cptr)(uintptr_t)wb_[((s) % K1_NS) >> 1] + (((s) % K1_NS) & 1) * K1_SLOT)
  LAS float* T = (LAS float*)(lds + K1_TAB);
  LAS float* cs = T;
  LAS float* st = T + 96 + w * 64;
  STAMP(0, 0);
  if (blockIdx.x == 0 && tid < 16) *(unsigned*)(ws + WS_SYNC + tid * 256) = 0;
  if (wid >= 8) {
    const unsigned lds0 = (unsigned)(uintptr_t)lds;
    const srd_t sx = SRD(x, SEQ * DM * 2), sw = SRD(wqkv, DM * 3 * DM * 2);
    const unsigned vW = (lane >> 2) * 3072 + (lane & 3) * 16;
    const unsigned vXa = (lane >> 3) * 1024 + (((lane & 7) ^ (lane >> 4)) * 16);
    const unsigned vXb = (lane >> 3) * 1024 + (((lane & 7) ^ (4 + (lane >> 4))) * 16);
    auto issue = [&](int s) {
      const unsigned sl = lds0 + (s % K1_NS) * K1_SLOT;
      const int sk = (s + (K1ROT)) & 7;
#pragma unroll
      for (int i = 0; i < 3; ++i) {
        const int idx = 3 * w + i, tile = idx >> 2, part = idx & 3;
        dma16(sl + 16384 + tile * 4096 + part * 1024, vW, sw, (64 * sk + 16 * part) * 3072 + tile * 1024 + g * 64);
      }
#pragma unroll
      for (int i = 0; i < 4; ++i) dma16(sl + (32 * w + 8 * i) * 128, (i & 1) ? vXb : vXa, sx, (tt * 32 + 8 * i) * 1024 + sk * 128);
    };
    asm volatile("s_nop 4");
    TR(0);
    issue(0); issue(1); issue(2);
#pragma unroll
    for (int s = 0; s < 8; ++s) {
      if (s <= 5) WAITV_BAR(14); else if (s == 6) WAITV_BAR(7); else WAITV_BAR(0);
      if (s + 3 < 8) issue(s + 3);
    }
    asm volatile("s_barrier" ::: "memory");
  } else if (wid >= 4) {
    const u32x4* xrow = (const u32x4*)(x + (size_t)(tt * 32 + tok) * DM + g * 32 + h * 8);
    const u32x4 xs0 = xrow[0], xs1 = xrow[2];
    f32x4 s1[2] = {}, s2[2] = {}, cc[2] = {};
    const bf16x8 ones = {0x3f80, 0x3f80, 0x3f80, 0x3f80, 0x3f80, 0x3f80, 0x3f80, 0x3f80};
    const int i16 = lane & 15, g4 = lane >> 4;
    unsigned xa_[3][4], wa_[3];
#pragma unroll
    for (int sp = 0; sp < 3; ++sp) {
#pragma unroll
      for (int u = 0; u < 4; ++u) { const int row = 32 * w + 16 * (u >> 1) + i16, c = 4 * (u & 1) + g4;
        xa_[sp][u] = (unsigned)(uintptr_t)L + row * 128 + ((c ^ ((row >> 1) & 7)) * 16) + sp * 2 * K1_SLOT; asm volatile("" : "+v"(xa_[sp][u])); }
      wa_[sp] = (unsigned)(uintptr_t)L + 16384 + (w < 3 ? w : 2) * 4096 + (8 * g4 + q) * 64 + p * 8 + sp * 2 * K1_SLOT; asm volatile("" : "+v"(wa_[sp]));
    }
    bf16x8 xa[2][4], wa[2][4];
#pragma unroll
    for (int s = 0; s < 9; ++s) {
      if (s < 8) {
        asm volatile("s_barrier" ::: "memory");
        const int sp = (s % K1_NS) >> 1, so = ((s % K1_NS) & 1) * K1_SLOT;
#pragma unroll
        for (int u = 0; u < 4; ++u) {
          xa[s & 1][u] = *(LAS const bf16x8*)((lds_cptr)(uintptr_t)xa_[sp][u] + so);
          wa[s & 1][u] = tr_frag((lds_cptr)(uintptr_t)wa_[sp] + so + (u >> 1) * 32 + (u & 1) * 2048, 256);
        }
      }
      __builtin_amdgcn_sched_barrier(0);
      if (s > 0) {
        const int b = (s - 1) & 1;
#pragma unroll
        for (int u = 0; u < 4; ++u) {
          s2[u >> 1] = __builtin_amdgcn_mfma_f32_16x16x32_bf16(xa[b][u], xa[b][u], s2[u >> 1], 0, 0, 0);
          s1[u >> 1] = __builtin_amdgcn_mfma_f32_16x16x32_bf16(xa[b][u], ones, s1[u >> 1], 0, 0, 0);
          cc[u >> 1] = __builtin_amdgcn_mfma_f32_16x16x32_bf16(wa[b][u], ones, cc[u >> 1], 0, 0, 0);
        }
      }
      __builtin_amdgcn_sched_barrier(0);
    }
    {
      const int r = i16 & 3;
      const bool diag = (i16 >> 2) == g4;
#pragma unroll
      for (int th = 0; th < 2; ++th) {
        const float a1 = r == 0 ? s1[th][0] : r == 1 ? s1[th][1] : r == 2 ? s1[th][2] : s1[th][3];
        const float a2 = r == 0 ? s2[th][0] : r == 1 ? s2[th][1] : r == 2 ? s2[th][2] : s2[th][3];
        const float mean = a1 * (1.0f / DM), var = __builtin_fmaxf(a2 * (1.0f / DM) - mean * mean, 0.f);
        const float rs = __builtin_amdgcn_rsqf(var + 1e-5f);
        if (diag) { st[16 * th + i16] = rs; st[32 + 16 * th + i16] = -mean * rs; }
        if (w < 3 && i16 == 0) *(LAS f32x4*)(cs + w * 32 + 16 * th + 4 * g4) = cc[th];
      }
    }
    asm volatile("s_waitcnt lgkmcnt(0)\n\ts_barrier" ::: "memory");
    const float rstd = st[tok], nm = st[32 + tok];
    char* Xi = ws + WS_XN;
    u32x4 y;
#pragma unroll
    for (int r = 0; r < 4; ++r) y[r] = cvtpk(bflo(xs0[r]) * rstd + nm, bfhi(xs0[r]) * rstd + nm);
    *(u32x4*)(Xi + (tt * 32 + 2 * g) * 1024 + lane * 16) = y;
#pragma unroll
    for (int r = 0; r < 4; ++r) y[r] = cvtpk(bflo(xs1[r]) * rstd + nm, bfhi(xs1[r]) * rstd + nm);
    *(u32x4*)(Xi + (tt * 32 + 2 * g + 1) * 1024 + lane * 16) = y;
  } else {
    f32x16 aq = {}, ak = {}, av = {};
    bf16x8 xf[3], wq[3], wk[3], wv[3];
#pragma unroll
    for (int n = 0; n < 34; ++n) {
      const int s = n >> 2, ks = n & 3, c = n % 3, m = (n + 1) % 3;
      if (n < 32 && ks == 0) { asm volatile("s_barrier" ::: "memory"); if (s == 0) STAMP(0, 1); }
      SB();
      if (n >= 2) aq = MFMA(wq[m], xf[m], aq);
      SB();
      if (n < 32) { xf[c] = *(LAS const bf16x8*)K1_XP(s, ks); wq[c] = tr_frag(K1_WP(s) + ks * 1024, 256); }
      SB();
      if (n >= 2) ak = MFMA(wk[m], xf[m], ak);
      SB();
      if (n < 32) wk[c] = tr_frag(K1_WP(s) + 4096 + ks * 1024, 256);
      SB();
      if (n >= 2) av = MFMA(xf[m], wv[m], av);
      SB();
      if (n < 32) wv[c] = tr_frag(K1_WP(s) + 8192 + ks * 1024, 256);
      SB();
    }
    STAMP(0, 2);
    asm volatile("s_barrier" ::: "memory");
    const float rstd = st[tok], nm = st[32 + tok], cv = cs[64 + tok];
    f32x4 cq4[4], ck4[4], r4[4], m4[4];
#pragma unroll
    for (int rg = 0; rg < 4; ++rg) { cq4[rg] = *(LAS const f32x4*)(cs + 8 * rg + 4 * h); ck4[rg] = *(LAS const f32x4*)(cs + 32 + 8 * rg + 4 * h);
      r4[rg] = *(LAS const f32x4*)(st + 8 * rg + 4 * h); m4[rg] = *(LAS const f32x4*)(st + 32 + 8 * rg + 4 * h); }
    const float QS = 0.25f * 1.4426950408889634f;
    char* Qi = ws + WS_Q; char* Ki = ws + WS_K; char* Vi = ws + WS_V;
    f32x16 o;
    const float rq = rstd * QS, nq = nm * QS;
#pragma unroll
    for (int r = 0; r < 16; ++r) o[r] = ak[r] * rstd + nm * ck4[r >> 2][r & 3];
    *(u32x4*)(Ki + ((2 * g) * 64 + tt) * 1024 + lane * 16) = pack8(o, 0);
    *(u32x4*)(Ki + ((2 * g + 1) * 64 + tt) * 1024 + lane * 16) = pack8(o, 8);
#pragma unroll
    for (int r = 0; r < 16; ++r) o[r] = av[r] * r4[r >> 2][r & 3] + m4[r >> 2][r & 3] * cv;
    char* vp = Vi + ((2 * g + grp1) * 64 + tt) * 1024 + h * 256 + (lane & 15) * 16;
    *(u32x4*)(vp) = pack8(o, 0);
    *(u32x4*)(vp + 512) = pack8(o, 8);
#pragma unroll
    for (int r = 0; r < 16; ++r) o[r] = aq[r] * rq + nq * cq4[r >> 2][r & 3];
    *(u32x4*)(Qi + ((2 * g) * 64 + tt) * 1024 + lane * 16) = pack8(o, 0);
    *(u32x4*)(Qi + ((2 * g + 1) * 64 + tt) * 1024 + lane * 16) = pack8(o, 8);
  }
  STAMP(0, 3);
}

constexpr float REF_OFF = 88.f;
#define ATT_UNROLL 8
#define STR2(x) #x
#define STR(x) STR2(x)
#define ATT_CLOB "v64","v65","v66","v67","v68","v69","v70","v71","v72","v73","v74","v75","v76","v77","v78","v79", \
  "v80","v81","v82","v83","v84","v85","v86","v87","v88","v89","v90","v91","v92","v93","v94","v95", \
  "v96","v97","v98","v99","v100","v101","v102","v103","v104","v105","v106","v107", \
  "v108","v109","v110","v111","v112","v113","v114","v115","v116","v117","v118","v119","v120","v121","v122","v123"
__device__ __forceinline__ void attn_fast(f32x16& o, const f32x16& c, const bf16x8& qf, unsigned kaddr, unsigned vaddr, unsigned cntaddr,
                                          srd_t sk, srd_t sv, unsigned voff, unsigned sL, unsigned sG) {
  const unsigned long long vmask = 0x0000ffff0000ffffull;
  const unsigned onev = (threadIdx.x & 15) == 0 ? 0x3f803f80u : 0u;
  asm volatile(
      ".macro ATT_DMA I\n"
      "s_add_u32 m0, %[sl], (((\\I)&1)*65536 + (2+((\\I)>>1))*8192)\n"
      "s_add_u32 s41, %[sg], ((2+((\\I)>>1))*8192)\n"
      ".if ((\\I)&1)\n"
      "buffer_load_dwordx4 %[vo], %[sv], s41 offen lds\n"
      ".else\n"
      "buffer_load_dwordx4 %[vo], %[sk], s41 offen lds\n"
      ".endif\n"
      ".endm\n"
      ".macro ATT_TILE X, Y, VC, VN, T, D\n"
      "s_waitcnt lgkmcnt(0)\n"
      "v_exp_f32 v[\\X+0], v[\\X+0]\n"
      "v_exp_f32 v[\\X+1], v[\\X+1]\n"
      "v_cvt_pk_bf16_f32 v102, v[\\Y+12], v[\\Y+13]\n"
      "v_exp_f32 v[\\X+2], v[\\X+2]\n"
      "v_cvt_pk_bf16_f32 v103, v[\\Y+14], v[\\Y+15]\n"
      "v_mfma_f32_32x32x16_bf16 v[\\Y:\\Y+15], v[104:107], %[q], %[c]\n"
      "ds_read_b128 v[104:107], %[ka] offset:(\\T+2)*1024\n"
      "v_exp_f32 v[\\X+3], v[\\X+3]\n"
      "v_cvt_pk_bf16_f32 v96, v[\\X+0], v[\\X+1]\n"
      "v_exp_f32 v[\\X+4], v[\\X+4]\n"
      "v_exp_f32 v[\\X+5], v[\\X+5]\n"
      "v_mfma_f32_32x32x16_bf16 %[o], v[\\VN+4:\\VN+7], v[100:103], %[o]\n"
      "s_mov_b64 exec, %[m]\n"
      "ds_read_b128 v[\\VN:\\VN+3], %[va] offset:(\\T+1)*1024\n"
      "ds_read_b128 v[\\VN+4:\\VN+7], %[va] offset:(\\T+1)*1024+512\n"
      "s_mov_b64 exec, -1\n"
      "v_cvt_pk_bf16_f32 v97, v[\\X+2], v[\\X+3]\n"
      "v_exp_f32 v[\\X+6], v[\\X+6]\n"
      ".if (\\D) >= 0\n ATT_DMA (\\D)\n .endif\n"
      "v_exp_f32 v[\\X+7], v[\\X+7]\n"
      "v_cvt_pk_bf16_f32 v98, v[\\X+4], v[\\X+5]\n"
      "v_exp_f32 v[\\X+8], v[\\X+8]\n"
      "v_exp_f32 v[\\X+9], v[\\X+9]\n"
      ".if (\\D) >= 0\n ATT_DMA (\\D)+1\n .endif\n"
      "v_cvt_pk_bf16_f32 v99, v[\\X+6], v[\\X+7]\n"
      "v_exp_f32 v[\\X+10], v[\\X+10]\n"
      "v_exp_f32 v[\\X+11], v[\\X+11]\n"
      "v_cvt_pk_bf16_f32 v100, v[\\X+8], v[\\X+9]\n"
      "v_mfma_f32_32x32x16_bf16 %[o], v[\\VC:\\VC+3], v[96:99], %[o]\n"
      "v_exp_f32 v[\\X+12], v[\\X+12]\n"
      "v_exp_f32 v[\\X+13], v[\\X+13]\n"
      ".if (\\D) >= 0\n ATT_DMA (\\D)+2\n .endif\n"
      "v_cvt_pk_bf16_f32 v101, v[\\X+10], v[\\X+11]\n"
      "v_exp_f32 v[\\X+14], v[\\X+14]\n"
      "v_exp_f32 v[\\X+15], v[\\X+15]\n"
      ".endm\n"
      ".set att_i, 108\n"
      ".rept 16\n"
      "v_mov_b32 v[att_i], %[one]\n"
      ".set att_i, att_i+1\n"
      ".endr\n"
      "v_mov_b32 v100, 0\n v_mov_b32 v101, 0\n v_mov_b32 v92, 0\n v_mov_b32 v93, 0\n v_mov_b32 v94, 0\n v_mov_b32 v95, 0\n"
      "ds_read_b128 v[104:107], %[ka]\n"
      "s_mov_b64 exec, %[m]\n"
      "ds_read_b128 v[108:111], %[va]\n"
      "ds_read_b128 v[112:115], %[va] offset:512\n"
      "s_mov_b64 exec, -1\n"
      "s_waitcnt lgkmcnt(2)\n"
      "v_mfma_f32_32x32x16_bf16 v[64:79], v[104:107], %[q], %[c]\n"
      "ds_read_b128 v[104:107], %[ka] offset:1024\n"
      "s_nop 7\n s_nop 7\n"
      "ATT_TILE 64, 80, 108, 116, 0, 0\n"
      "ATT_TILE 80, 64, 116, 108, 1, 3\n"
      "ATT_TILE 64, 80, 108, 116, 2, 6\n"
      "ATT_TILE 80, 64, 116, 108, 3, 9\n"
      "ATT_TILE 64, 80, 108, 116, 4, -1\n"
      "ATT_TILE 80, 64, 116, 108, 5, -1\n"
      "ATT_TILE 64, 80, 108, 116, 6, -1\n"
      "ATT_TILE 80, 64, 116, 108, 7, -1\n"
      "v_add_u32 %[ka], 8192, %[ka]\n"
      "v_add_u32 %[va], 8192, %[va]\n"
      "s_waitcnt vmcnt(0)\n"
      "v_mov_b32 v124, 1\n"
      "s_mov_b64 exec, 1\n"
      "ds_add_u32 %[cn], v124\n"
      "s_mov_b64 exec, -1\n"
      "s_mov_b32 s41, 0\n"
      "att_poll%=:\n"
      "ds_read_b32 v124, %[cn]\n"
      "s_add_u32 s41, s41, 1\n"
      "s_cmp_gt_u32 s41, 0x40000\n"
      "s_cbranch_scc1 att_go%=\n"
      "s_waitcnt lgkmcnt(0)\n"
      "v_readfirstlane_b32 s42, v124\n"
      "s_nop 0\n"
      "s_cmp_ge_u32 s42, 8\n"
      "s_cbranch_scc0 att_poll%=\n"
      "att_go%=:\n"
      "s_movk_i32 s40, 7\n"
      "att_loop%=:\n"
      ".set att_t, 0\n"
      ".rept 4\n"
      "ATT_TILE 64, 80, 108, 116, att_t, -1\n"
      "ATT_TILE 80, 64, 116, 108, att_t+1, -1\n"
      ".set att_t, att_t+2\n"
      ".endr\n"
      "v_add_u32 %[ka], 8192, %[ka]\n"
      "v_add_u32 %[va], 8192, %[va]\n"
      "s_sub_u32 s40, s40, 1\n"
      "s_cmp_lg_u32 s40, 0\n"
      "s_cbranch_scc1 att_loop%=\n"
      "s_waitcnt lgkmcnt(0)\n"
      "v_cvt_pk_bf16_f32 v102, v92, v93\n"
      "v_cvt_pk_bf16_f32 v103, v94, v95\n"
      "s_nop 1\n"
      "v_mfma_f32_32x32x16_bf16 %[o], v[120:123], v[100:103], %[o]\n"
      "s_nop 7\n s_nop 7\n"
      ".purgem ATT_TILE\n"
      ".purgem ATT_DMA\n"
      : [o] "+v"(o), [ka] "+v"(kaddr), [va] "+v"(vaddr)
      : [c] "v"(c), [q] "v"(qf), [m] "s"(vmask), [one] "v"(onev), [cn] "v"(cntaddr), [sk] "s"(sk), [sv] "s"(sv), [vo] "v"(voff), [sl] "s"(sL), [sg] "s"(sG)
      : "memory", "s40", "s41", "s42", "scc", "v124", ATT_CLOB);
}

__device__ __forceinline__ void attn_safe(f32x16& o, bf16x8 qf, lds_cptr kp, lds_cptr vp, int lane) {
  o = f32x16{};
  float m = -INFINITY;
  const bf16x8 ones = {0x3f80, 0x3f80, 0x3f80, 0x3f80, 0x3f80, 0x3f80, 0x3f80, 0x3f80};
#pragma unroll 1
  for (int t = 0; t < NTT; ++t) {
    const bf16x8 kf = *(LAS const bf16x8*)(kp + t * 1024);
    f32x16 s = MFMA(kf, qf, f32x16{});
    float mx = s[0];
#pragma unroll
    for (int r = 1; r < 16; ++r) mx = __builtin_fmaxf(mx, s[r]);
    mx = __builtin_fmaxf(half_max(mx), m);
    const float a = __builtin_amdgcn_exp2f(m - mx);
    m = mx;
#pragma unroll
    for (int r = 0; r < 16; ++r) { o[r] *= a; s[r] = __builtin_amdgcn_exp2f(s[r] - m); }
    bf16x8 v0 = *(LAS const bf16x8*)(vp + t * 1024), v1 = *(LAS const bf16x8*)(vp + t * 1024 + 512);
    if (lane & 16) { v0 = ones; v1 = ones; }
    o = MFMA(v0, __builtin_bit_cast(bf16x8, pack8(s, 0)), o);
    o = MFMA(v1, __builtin_bit_cast(bf16x8, pack8(s, 8)), o);
  }
}

__device__ __forceinline__ u32x4 attn_wave(const char* lds, bf16x8 qf, int lane, unsigned cntaddr, srd_t sk, srd_t sv, unsigned sL, unsigned sG) {
  const lds_cptr kp = (lds_cptr)lds + lane * 16;
  const lds_cptr vp = (lds_cptr)lds + 65536 + (lane >> 5) * 256 + (lane & 15) * 16;
  float mx;
  {
    const f32x16 s0 = MFMA(*(LAS const bf16x8*)(kp), qf, f32x16{}), s1 = MFMA(*(LAS const bf16x8*)(kp + 1024), qf, f32x16{});
    mx = __builtin_fmaxf(s0[0], s1[0]);
#pragma unroll
    for (int r = 1; r < 16; ++r) mx = __builtin_fmaxf(__builtin_fmaxf(mx, s0[r]), s1[r]);
    mx = half_max(mx);
  }
  const float nref = -(mx + REF_OFF);
  f32x16 c, o = {};
#pragma unroll
  for (int r = 0; r < 16; ++r) c[r] = nref;
  attn_fast(o, c, qf, (unsigned)(uintptr_t)kp, (unsigned)(uintptr_t)vp, cntaddr, sk, sv, lane * 16, sL, sG);
  STAMP(1, 2);
  { auto r = __builtin_amdgcn_permlane32_swap(__float_as_uint(o[8]), __float_as_uint(o[8]), false, false); o[8] = __uint_as_float(r[0]); }
  float amax = 0.f; bool bad = !(o[8] < 0x1p126f);
#pragma unroll
  for (int r = 0; r < 8; ++r) { const float a = __builtin_fabsf(o[r]); bad |= !(a < 0x1p126f); amax = __builtin_fmaxf(amax, a); }
  bad |= !(amax > 0x1p-100f);
  if (__builtin_expect(__any(bad), 0)) attn_safe(o, qf, kp, vp, lane);
  const float rl = 1.0f / o[8];
#pragma unroll
  for (int r = 0; r < 8; ++r) o[r] *= rl;
  return pack8(o, 0);
}

constexpr int F_BASE = 131072, F_SLOT = 6400, F_NS = 5, F_CNT = F_BASE + 32512;
constexpr int CNT_KV = F_CNT, CNT_F = F_CNT + 64, CNT_UP = F_CNT + 96;
__device__ __forceinline__ float nopk(float v) { asm("" : "+v"(v)); return v; }
__device__ __forceinline__ float silu(float v) { return v * __builtin_amdgcn_rcpf(1.0f + __builtin_amdgcn_exp2f(-1.4426950408889634f * v)); }
#define FPRIO 2
#define POLLLO 1
#define POLL_PRIO_LO "s_setprio 0\n"
#define POLL_PRIO_HI "s_setprio " STR(FPRIO) "\n"
__device__ __forceinline__ void fbar(unsigned cntaddr, unsigned target) {
  unsigned t, sv, sp;
  asm volatile(
      "v_mov_b32 %0, 1\n"
      "s_mov_b64 exec, 1\n"
      "ds_add_u32 %3, %0\n"
      "s_mov_b64 exec, -1\n"
      "s_mov_b32 %2, 0\n"
      POLL_PRIO_LO
      "fb_poll%=:\n"
      "ds_read_b32 %0, %3\n"
      "s_add_u32 %2, %2, 1\n"
      "s_cmp_gt_u32 %2, 0x40000\n"
      "s_cbranch_scc1 fb_done%=\n"
      "s_waitcnt lgkmcnt(0)\n"
      "v_readfirstlane_b32 %1, %0\n"
      "s_nop 0\n"
      "s_cmp_ge_u32 %1, %4\n"
      "s_cbranch_scc0 fb_poll%=\n"
      "fb_done%=:\n"
      POLL_PRIO_HI
      "s_waitcnt lgkmcnt(0)\n"
      : "=&v"(t), "=&s"(sv), "=&s"(sp) : "v"(cntaddr), "s"(target) : "memory", "scc");
}
__device__ __forceinline__ void f_arrive(unsigned cntaddr) {
  unsigned t;
  asm volatile("v_mov_b32 %0, 1\n\ts_mov_b64 exec, 1\n\tds_add_u32 %1, %0\n\ts_mov_b64 exec, -1" : "=&v"(t) : "v"(cntaddr) : "memory");
}
__device__ __forceinline__ void f_check(unsigned cntaddr, unsigned target) {
  unsigned t, sv, sp;
  asm volatile(
      "s_mov_b32 %2, 0\n"
      "fc_poll%=:\n"
      "ds_read_b32 %0, %3\n"
      "s_add_u32 %2, %2, 1\n"
      "s_cmp_gt_u32 %2, 0x40000\n"
      "s_cbranch_scc1 fc_done%=\n"
      "s_waitcnt lgkmcnt(0)\n"
      "v_readfirstlane_b32 %1, %0\n"
      "s_nop 0\n"
      "s_cmp_ge_u32 %1, %4\n"
      "s_cbranch_scc0 fc_poll%=\n"
      "fc_done%=:\n"
      "s_waitcnt lgkmcnt(0)\n"
      : "=&v"(t), "=&s"(sv), "=&s"(sp) : "v"(cntaddr), "s"(target) : "memory", "scc");
}
__device__ __forceinline__ void dma16_48(unsigned lds_base, unsigned voff, srd_t srd, unsigned soff) {
  const unsigned long long m48 = 0xffffffffffffull;
  asm volatile("s_mov_b32 m0, %0\n\ts_mov_b64 exec, %4\n\tbuffer_load_dwordx4 %1, %2, %3 offen lds\n\ts_mov_b64 exec, -1" ::"s"(lds_base), "v"(voff), "s"(srd), "s"(soff), "s"(m48) : "memory");
}
#define FSLEEP 1
#define DSLEEP 3
#define SLP(n) do { SB(); __builtin_amdgcn_s_sleep(n); SB(); } while (0)
#define FPACE() SLP(1)
#define FPACE_A() SLP(1)
#define DPACE() SLP(1)
#define DPACE_A()
template <int AUX>
__device__ __forceinline__ void store16(srd_t srd, unsigned voff, unsigned soff, u32x4 v) { __builtin_amdgcn_raw_buffer_store_b128(v, srd, voff, soff, AUX); }

__device__ __forceinline__ void ffn_up(char* lds, const u16* w1, const u16* w3, char* ws, int panel, int gi, int w, int lane, unsigned& nbar) {
  const unsigned lds0 = (unsigned)(uintptr_t)lds, cnt = lds0 + CNT_F;
  const int tt0 = 8 * panel + 2 * w, h = lane >> 5;
  const int ntile = gi < 22 ? 3 : 2, t0 = gi < 22 ? 3 * gi : 66 + 2 * (gi - 22);
  const srd_t s1 = SRD(w1, DM * FF * 2), s3 = SRD(w3, DM * FF * 2), sx = SRD(ws + WS_XN, IMG_BYTES), sh = SRD(ws + WS_H, H_BYTES);
  const unsigned vw = (lane / 6) * (FF * 2) + (lane % 6) * 16, vx = lane * 16;
  auto issueW = [&](int j) {
    const unsigned so = (32 * j + 8 * w) * (FF * 2) + t0 * 32, dst = lds0 + F_BASE + (j % F_NS) * F_SLOT + w * 768;
    dma16_48(dst, vw, s1, so);
    dma16_48(dst + 3200, vw, s3, so);
  };
  u32x4 xr[2][4];
  auto issueX = [&](int j) {
    const unsigned so = (tt0 * 32 + 2 * j) * 1024;
    asm volatile("buffer_load_dwordx4 %0, %4, %5, %6 offen\n\tbuffer_load_dwordx4 %1, %4, %5, %7 offen\n\t"
                 "buffer_load_dwordx4 %2, %4, %5, %6 offen offset:1024\n\tbuffer_load_dwordx4 %3, %4, %5, %7 offen offset:1024"
                 : "=&v"(xr[j & 1][0]), "=&v"(xr[j & 1][1]), "=&v"(xr[j & 1][2]), "=&v"(xr[j & 1][3]) : "v"(vx), "s"(sx), "s"(so), "s"(so + 32768) : "memory");
  };
  const int grp1 = (lane >> 4) & 1, q = (lane & 15) >> 2, p = lane & 3;
  const lds_cptr wb = (lds_cptr)lds + F_BASE + (grp1 ? 3200 : 0) + (8 * h + q) * 96 + 8 * p;
  asm volatile("s_nop 4");
  issueX(0); issueW(0); issueW(1); issueX(1); issueW(2);
  asm volatile("s_waitcnt lgkmcnt(0)\n\ts_barrier" ::: "memory");
  f32x16 acc[2][3] = {};
  bf16x8 wf[2][3];
  const unsigned cu = lds0 + CNT_UP;
#define XWAIT(n, b) asm volatile("s_waitcnt vmcnt(%4) lgkmcnt(0)" : "+v"(xr[b][0]), "+v"(xr[b][1]), "+v"(xr[b][2]), "+v"(xr[b][3]) : "i"(n) : "memory")
  XWAIT(6, 0);
  f_arrive(cu); f_arrive(cu);
  f_check(cu, 8);
#pragma unroll
  for (int T = 0; T < 3; ++T) wf[0][T] = tr_frag(wb + T * 32, 384);
#pragma unroll
  for (int j = 0; j < 16; ++j) {
    if (j == 1) XWAIT(6, 1);
    else if (j >= 2) XWAIT((j + 2 <= 15 ? 2 : 0) + (j + 1 <= 15 ? 4 : 0), j & 1);
    if (j >= 1 && j + 1 <= 15) f_arrive(cu);
    SB();
#pragma unroll
    for (int T = 0; T < 3; ++T) {
      acc[0][T] = MFMA(wf[0][T], __builtin_bit_cast(bf16x8, xr[j & 1][0]), acc[0][T]);
      SB();
      wf[1][T] = tr_frag(wb + (j % F_NS) * F_SLOT + 1536 + T * 32, 384);
      FPACE_A();
      acc[1][T] = MFMA(wf[0][T], __builtin_bit_cast(bf16x8, xr[j & 1][1]), acc[1][T]);
      if (T < 2) FPACE();
    }
    SB();
    if (j < 15) {
      if (j >= 1) f_check(cu, 4 * (j + 2));
      if (j + 3 < 16) issueW(j + 3);
    }
    SB();
#pragma unroll
    for (int T = 0; T < 3; ++T) {
      acc[0][T] = MFMA(wf[1][T], __builtin_bit_cast(bf16x8, xr[j & 1][2]), acc[0][T]);
      FPACE_A();
      acc[1][T] = MFMA(wf[1][T], __builtin_bit_cast(bf16x8, xr[j & 1][3]), acc[1][T]);
      SB();
      if (j < 15) wf[0][T] = tr_frag(wb + ((j + 1) % F_NS) * F_SLOT + T * 32, 384);
      if (T < 2) FPACE();
    }
    SB();
    if (j + 2 < 16) issueX(j + 2);
    SB();
  }
#pragma unroll
  for (int T = 0; T < 3; ++T)
#pragma unroll
    for (int th = 0; th < 2; ++th) {
      f32x16 hv;
#pragma unroll
      for (int r = 0; r < 8; ++r) hv[r] = nopk(silu(acc[th][T][r]) * acc[th][T][8 + r]);
      if (T < ntile) store16<16>(sh, lane * 16, ((tt0 + th) * NFG + t0 + T) * 1024, pack8(hv, 0));
    }
}

typedef __attribute__((address_space(1))) unsigned gu32;
__device__ __forceinline__ void wait_count(gu32* ctr, unsigned want) {
  for (unsigned spins = 0; spins < (1u << 16); ++spins) {
    if ((unsigned)__builtin_amdgcn_readfirstlane(__hip_atomic_load(ctr, __ATOMIC_RELAXED, __HIP_MEMORY_SCOPE_AGENT)) >= want) break;
    __builtin_amdgcn_s_sleep(2);
  }
}
__device__ __forceinline__ void acquire_agent() {
  __builtin_amdgcn_fence(__ATOMIC_ACQUIRE, "agent");
  asm volatile("s_waitcnt vmcnt(0)" ::: "memory");
}
__device__ __forceinline__ void lds_wait(unsigned addr, unsigned want) {
  for (unsigned spins = 0; spins < (1u << 18); ++spins) {
    unsigned v; asm volatile("ds_read_b32 %0, %1\n\ts_waitcnt lgkmcnt(0)" : "=&v"(v) : "v"(addr) : "memory");
    if ((unsigned)__builtin_amdgcn_readfirstlane(v) >= want) break;
    __builtin_amdgcn_s_sleep(4);
  }
}

constexpr int DN_NS = 3, DN_HD = 5;
__device__ __forceinline__ void ffn_down(char* lds, const u16* w2, char* ws, int tb, int nb, int w, int lane, f32x16& p00, f32x16& p01, f32x16& p10, f32x16& p11) {
  const unsigned lbase = (unsigned)(uintptr_t)lds, lds0 = lbase + F_BASE + w * (DN_NS * 2048);
  const int h = lane >> 5;
  const int k0 = w < 2 ? 22 * w : 44 + 21 * (w - 2), nst = w < 2 ? 22 : 21;
  const srd_t s2 = SRD(w2, FF * DM * 2), sh = SRD(ws + WS_H, H_BYTES);
  const unsigned vw = (lane >> 3) * 1024 + (((lane & 7) ^ (((lane >> 4) & 1) << 2)) * 16), vh = lane * 16;
  const int grp1 = (lane >> 4) & 1, q = (lane & 15) >> 2, p = lane & 3;
  const lds_cptr L = (lds_cptr)lds + F_BASE + w * (DN_NS * 2048);
  const lds_cptr b0 = L + (4 * h + q) * 128 + ((q >> 1) & 1) * 64 + grp1 * 32 + p * 8;
  const lds_cptr b1 = L + (4 * h + q) * 128 + (((q >> 1) & 1) ^ 1) * 64 + grp1 * 32 + p * 8;
  u32x4 hf[DN_HD][2];
  auto issueW = [&](int j) {
    const unsigned so = (k0 + (j < nst ? j : 0)) * 16 * 1024 + nb * 128;
    dma16(lds0 + (j % DN_NS) * 2048, vw, s2, so);
    dma16(lds0 + (j % DN_NS) * 2048 + 1024, vw, s2, so + 8192);
  };
  auto issueH = [&](int j) {
    const unsigned ho = j < nst ? (2 * tb * NFG + k0 + j) * 1024 : 0x7ff00000u;
    asm volatile("buffer_load_dwordx4 %0, %2, %3, %4 offen sc1\n\tbuffer_load_dwordx4 %1, %2, %3, %5 offen sc1"
                 : "=&v"(hf[j % DN_HD][0]), "=&v"(hf[j % DN_HD][1]) : "v"(vh), "s"(sh), "s"(ho), "s"(ho + (j < nst ? NFG * 1024 : 0)) : "memory");
  };
  asm volatile("s_nop 4");
  issueH(0); issueH(1); issueW(0); issueH(2); issueW(1); issueH(3); issueW(2);
  f32x16 a00 = {}, a01 = {}, a10 = {}, a11 = {};
  bf16x8 w0[2], w1f[2];
  asm volatile("s_waitcnt vmcnt(8)" : "+v"(hf[0][0]), "+v"(hf[0][1])::"memory");
  w0[0] = tr_frag(b0, 1024); w1f[0] = tr_frag(b1, 1024);
#pragma unroll
  for (int j = 0; j < 22; ++j) {
    const int c = j & 1, n = c ^ 1;
    if (j + 1 < 22) {
      const int young = j < 2 ? 4 : (j + 2 < 22 ? 4 : 0) + (j + 3 < 22 ? 2 : 0);
      asm volatile("s_waitcnt vmcnt(%2) lgkmcnt(0)" : "+v"(hf[(j + 1) % DN_HD][0]), "+v"(hf[(j + 1) % DN_HD][1]) : "i"(young) : "memory");
    } else asm volatile("s_waitcnt lgkmcnt(0)" ::: "memory");
    if (j + 3 < 22) issueW(j + 3);
    if (j + 4 < 22) issueH(j + 4);
    const bf16x8 h0 = __builtin_bit_cast(bf16x8, hf[j % DN_HD][0]), h1 = __builtin_bit_cast(bf16x8, hf[j % DN_HD][1]);
    SB();
    a00 = MFMA(w0[c], h0, a00);
    SB();
    if (j + 1 < 22) w0[n] = tr_frag(b0 + ((j + 1) % DN_NS) * 2048, 1024);
    DPACE_A();
    a01 = MFMA(w0[c], h1, a01);
    DPACE();
    a10 = MFMA(w1f[c], h0, a10);
    SB();
    if (j + 1 < 22) w1f[n] = tr_frag(b1 + ((j + 1) % DN_NS) * 2048, 1024);
    DPACE_A();
    a11 = MFMA(w1f[c], h1, a11);
    DPACE();
  }
  p00 = a00; p01 = a01; p10 = a10; p11 = a11;
}

__device__ __forceinline__ void final_tile(char* lds, const u16* x, const u16* wo, char* ws, u16* out, const f32x16& a00, const f32x16& a01, const f32x16& a10, const f32x16& a11,
                                           int panel, int tb, int nb, int w, int lane, unsigned& nbar) {
  const unsigned lds0 = (unsigned)(uintptr_t)lds, cnt = lds0 + CNT_F;
  const int nh = w >> 1, th = w & 1, tt = 2 * tb + th, h = lane >> 5;
  const srd_t so = SRD(wo, DM * DM * 2), sO = SRD(ws + WS_O, IMG_BYTES);
  const size_t ob = (size_t)(tt * 32 + (lane & 31)) * DM + nb * 64 + nh * 32 + 4 * h;
  u32x2 xv[4];
#pragma unroll
  for (int i = 0; i < 4; ++i) xv[i] = *(const u32x2*)(x + ob + 8 * i);
  lds_wait(lds0 + CNT_KV + 4, 8);
  const unsigned vw = (lane >> 3) * 1024 + (((lane & 7) ^ (((lane >> 4) & 1) << 2)) * 16);
#pragma unroll
  for (int i = 0; i < 16; ++i) dma16(lds0 + (4 * i + w) * 1024, vw, so, (4 * i + w) * 8192 + nb * 128);
  LAS f32x4* R = (LAS f32x4*)(lds + 65536);
#pragma unroll
  for (int qd = 0; qd < 4; ++qd) {
    const f32x16& snd = qd == 0 ? a00 : qd == 1 ? a01 : qd == 2 ? a10 : a11;
#pragma unroll
    for (int i = 0; i < 4; ++i) R[((qd * 4 + w) * 4 + i) * 64 + lane] = (f32x4){snd[4 * i], snd[4 * i + 1], snd[4 * i + 2], snd[4 * i + 3]};
  }
  asm volatile("s_waitcnt lgkmcnt(0)" ::: "memory");
  nbar += 4; fbar(cnt, nbar);
  f32x16 g;
#pragma unroll
  for (int i = 0; i < 4; ++i) {
    const f32x4 v0 = R[((w * 4 + 0) * 4 + i) * 64 + lane], v1 = R[((w * 4 + 1) * 4 + i) * 64 + lane], v2 = R[((w * 4 + 2) * 4 + i) * 64 + lane], v3 = R[((w * 4 + 3) * 4 + i) * 64 + lane];
#pragma unroll
    for (int e = 0; e < 4; ++e) g[4 * i + e] = silu((v0[e] + v1[e]) + (v2[e] + v3[e]));
  }
  const u32x4 g0 = pack8(g, 0), g1 = pack8(g, 8);
  if (w == 0) { wait_count((gu32*)(ws + WS_SYNC + (8 + panel) * 256), 32); }
  asm volatile("s_waitcnt vmcnt(0) lgkmcnt(0)" ::: "memory");
  nbar += 4; fbar(cnt, nbar);
  STAMP(2, 2);
  bf16x8 of[32];
#pragma unroll
  for (int hd = 0; hd < 32; ++hd) of[hd] = __builtin_bit_cast(bf16x8, __builtin_amdgcn_raw_buffer_load_b128(sO, lane * 16, (tt * 32 + hd) * 1024, 16));
  const int grp1 = (lane >> 4) & 1, q = (lane & 15) >> 2, p = lane & 3;
  const lds_cptr b = (lds_cptr)lds + (4 * h + q) * 128 + ((((q >> 1) & 1) ^ nh) * 64) + grp1 * 32 + p * 8;
  f32x16 a = {};
#pragma unroll
  for (int hd = 0; hd < 32; ++hd) a = MFMA(tr_frag(b + hd * 2048, 1024), of[hd], a);
#pragma unroll
  for (int i = 0; i < 4; ++i) {
    const unsigned ga = i < 2 ? g0[2 * i] : g1[2 * i - 4], gb = i < 2 ? g0[2 * i + 1] : g1[2 * i - 3];
    u32x2 y;
    y[0] = cvtpk(bflo(xv[i][0]) + a[4 * i] * bflo(ga), bfhi(xv[i][0]) + a[4 * i + 1] * bfhi(ga));
    y[1] = cvtpk(bflo(xv[i][1]) + a[4 * i + 2] * bflo(gb), bfhi(xv[i][1]) + a[4 * i + 3] * bfhi(gb));
    *(u32x2*)(out + ob + 8 * i) = y;
  }
}

constexpr int K2_LDS = 163840;
__global__ __launch_bounds__(768) void k_main(const u16* __restrict__ x, const u16* __restrict__ wo, const u16* __restrict__ w1, const u16* __restrict__ w3, const u16* __restrict__ w2,
                                              char* __restrict__ ws, u16* __restrict__ out) {
  extern __shared__ __attribute__((aligned(16))) char lds[];
  const int tid = threadIdx.x, lane = tid & 63;
  const int wid = __builtin_amdgcn_readfirstlane(tid >> 6);
  const int xcd = blockIdx.x & 7, bi = blockIdx.x >> 3;
  const unsigned lds0 = (unsigned)(uintptr_t)lds;
  if (wid < 8) {
    const int w = wid, hd = 4 * xcd + (bi >> 3), qb = bi & 7, tt = qb * 8 + w;
    const srd_t sk = SRD(ws + WS_K, IMG_BYTES), sv = SRD(ws + WS_V, IMG_BYTES), sq = SRD(ws + WS_Q, IMG_BYTES);
    STAMP(1, 0);
    u32x4 qv;
    asm volatile("s_nop 4\n\tbuffer_load_dwordx4 %0, %1, %2, %3 offen" : "=&v"(qv) : "v"(lane * 16), "s"(sq), "s"((hd * 64 + tt) * 1024) : "memory");
#pragma unroll
    for (int c = 0; c < 2; ++c) {
      dma16(lds0 + (c * 8 + w) * 1024, lane * 16, sk, hd * 65536 + (c * 8 + w) * 1024);
      dma16(lds0 + 65536 + (c * 8 + w) * 1024, lane * 16, sv, hd * 65536 + (c * 8 + w) * 1024);
    }
    asm volatile("s_waitcnt vmcnt(0)\n\ts_barrier" : "+v"(qv)::"memory");
    STAMP(1, 1);
    const u32x4 ov = attn_wave(lds, __builtin_bit_cast(bf16x8, qv), lane, lds0 + CNT_KV, sk, sv, lds0 + w * 1024, hd * 65536 + w * 1024);
    store16<16>(SRD(ws + WS_O, IMG_BYTES), lane * 16, (tt * 32 + hd) * 1024, ov);
    asm volatile("s_waitcnt vmcnt(0) lgkmcnt(0)" ::: "memory");
    unsigned old = 0;
    if (lane == 0) old = __hip_atomic_fetch_add((LAS unsigned*)(lds + CNT_KV + 4), 1u, __ATOMIC_RELAXED, __HIP_MEMORY_SCOPE_WORKGROUP);
    if (__builtin_amdgcn_readfirstlane(old) == 7 && lane == 0) __hip_atomic_fetch_add((gu32*)(ws + WS_SYNC + (8 + qb) * 256), 1u, __ATOMIC_RELAXED, __HIP_MEMORY_SCOPE_AGENT);
    STAMP(1, 3);
  } else {
    const int w = wid - 8;
    if (w == 0 && lane < 32) *(LAS unsigned*)(lds + F_CNT + lane * 4) = 0;
    if (FPRIO) __builtin_amdgcn_s_setprio(FPRIO);
    unsigned nbar = 0;
    STAMP(2, 0);
    ffn_up(lds, w1, w3, ws, xcd, bi, w, lane, nbar);
    asm volatile("s_waitcnt vmcnt(0) lgkmcnt(0)" ::: "memory");
    nbar += 4; fbar(lds0 + CNT_F, nbar);
    if (w == 0) {
      if (lane == 0) __hip_atomic_fetch_add((gu32*)(ws + WS_SYNC + xcd * 256), 1u, __ATOMIC_RELAXED, __HIP_MEMORY_SCOPE_AGENT);
      wait_count((gu32*)(ws + WS_SYNC + xcd * 256), 32);
    }
    nbar += 4; fbar(lds0 + CNT_F, nbar);
    STAMP(2, 1);
    const int tb = 4 * xcd + (bi >> 3), nb = bi & 7;
    f32x16 p00, p01, p10, p11;
    ffn_down(lds, w2, ws, tb, nb, w, lane, p00, p01, p10, p11);
    STAMP(3, 0);
    final_tile(lds, x, wo, ws, out, p00, p01, p10, p11, xcd, tb, nb, w, lane, nbar);
    STAMP(2, 3);
  }
}

extern "C" void kernel_launch(void* const* d_in, const int* in_sizes, int n_in, void* d_out, int out_size, void* d_ws, size_t ws_size, hipStream_t stream) {
  const u16* x = (const u16*)d_in[0]; const u16* wqkv = (const u16*)d_in[1]; const u16* wo = (const u16*)d_in[2];
  const u16* w1 = (const u16*)d_in[4]; const u16* w3 = (const u16*)d_in[5]; const u16* w2 = (const u16*)d_in[6];
  char* ws = (char*)d_ws;
  (void)hipFuncSetAttribute((const void*)k_qkv, hipFuncAttributeMaxDynamicSharedMemorySize, K1_LDS);
  (void)hipFuncSetAttribute((const void*)k_main, hipFuncAttributeMaxDynamicSharedMemorySize, K2_LDS);
  k_qkv<<<256, 768, K1_LDS, stream>>>(x, wqkv, ws);
  k_main<<<256, 768, K2_LDS, stream>>>(x, wo, w1, w3, w2, ws, (u16*)d_out);
}
```

```cpp
#include <hip/hip_runtime.h>
#include <hip/hip_bf16.h>
#include <stdint.h>

typedef unsigned short u16;
typedef short s16x4 __attribute__((ext_vector_type(4)));
typedef short bf16x8 __attribute__((ext_vector_type(8)));
typedef float f32x16 __attribute__((ext_vector_type(16)));
typedef float f32x4 __attribute__((ext_vector_type(4)));
typedef unsigned u32x4 __attribute__((ext_vector_type(4)));
typedef unsigned u32x2 __attribute__((ext_vector_type(2)));
typedef __bf16 bf16x2 __attribute__((ext_vector_type(2)));
#define LAS __attribute__((address_space(3)))
typedef LAS const char* lds_cptr;
typedef LAS char* lds_ptr;

constexpr int SEQ = 2048, DM = 512, NH = 32, FF = 1376;
constexpr int NTT = 64;
constexpr int NFG = 86;
constexpr size_t WS_Q = 0, WS_K = 2u << 20, WS_V = 4u << 20, WS_XN = 6u << 20, WS_O = 8u << 20, WS_G = 10u << 20, WS_H = 12u << 20;
constexpr size_t WS_SYNC = 20u << 20, WS_FQ = WS_SYNC + 4096, WS_FH = WS_SYNC + 8192, WS_FO = WS_SYNC + 12288;
constexpr unsigned IMG_BYTES = 2u << 20, H_BYTES = NTT * NFG * 1024;

constexpr int F_BASE = 131072, F_SLOT = 6400, F_NS = 5, F_CNT = F_BASE + 32512;
constexpr int CNT_KV = F_CNT, CNT_F = F_CNT + 64, CNT_UP = F_CNT + 96, TAG_W = F_CNT + 112;
#define MFMA(a, b, c) __builtin_amdgcn_mfma_f32_32x32x16_bf16(a, b, c, 0, 0, 0)
#define SRD(p, bytes) __builtin_amdgcn_make_buffer_rsrc((void*)(p), (short)0, (int)(bytes), 0x00020000)
typedef __amdgpu_buffer_rsrc_t srd_t;

__device__ __forceinline__ void dma16(unsigned lds_base, unsigned voff, srd_t srd, unsigned soff) {
  asm volatile("s_mov_b32 m0, %0\n\ts_nop 0\n\tbuffer_load_dwordx4 %1, %2, %3 offen lds" ::"s"(lds_base), "v"(voff), "s"(srd), "s"(soff) : "memory");
}
__device__ __forceinline__ void dma16_sc1(unsigned lds_base, unsigned voff, srd_t srd, unsigned soff) {
  asm volatile("s_mov_b32 m0, %0\n\ts_nop 0\n\tbuffer_load_dwordx4 %1, %2, %3 offen sc1 lds" ::"s"(lds_base), "v"(voff), "s"(srd), "s"(soff) : "memory");
}
template <int AUX>
__device__ __forceinline__ void store16(srd_t srd, unsigned voff, unsigned soff, u32x4 v) { __builtin_amdgcn_raw_buffer_store_b128(v, srd, voff + soff, 0, AUX); }
typedef __attribute__((address_space(1))) unsigned long long gu64;
typedef unsigned long long u64;
__device__ __forceinline__ void flag_store(char* p, u64 tag) { __hip_atomic_store((gu64*)p, tag, __ATOMIC_RELAXED, __HIP_MEMORY_SCOPE_AGENT); }
__device__ __forceinline__ void flags_wait(const char* p, bool mine, u64 tag) {
  for (unsigned spins = 0; spins < (1u << 16); ++spins) {
    const u64 v = __hip_atomic_load((gu64*)p, __ATOMIC_RELAXED, __HIP_MEMORY_SCOPE_AGENT);
    if (__all(!mine || v == tag)) break;
    __builtin_amdgcn_s_sleep(1);
  }
}
__device__ __forceinline__ s16x4 tr_read(lds_cptr p) {
  return __builtin_bit_cast(s16x4, __builtin_amdgcn_ds_read_tr16_b64_v4i16((LAS s16x4*)p));
}
__device__ __forceinline__ bf16x8 tr_frag(lds_cptr p, int second) {
  s16x4 lo = tr_read(p), hi = tr_read(p + second);
  return (bf16x8){lo[0], lo[1], lo[2], lo[3], hi[0], hi[1], hi[2], hi[3]};
}
__device__ __forceinline__ unsigned cvtpk(float lo, float hi) {
  bf16x2 r = {(__bf16)lo, (__bf16)hi};
  return __builtin_bit_cast(unsigned, r);
}
__device__ __forceinline__ float dot2(unsigned a, unsigned b, float c) {
  return __builtin_amdgcn_fdot2_f32_bf16(__builtin_bit_cast(bf16x2, a), __builtin_bit_cast(bf16x2, b), c, false);
}
__device__ __forceinline__ float bflo(unsigned v) { return __uint_as_float(v << 16); }
__device__ __forceinline__ float bfhi(unsigned v) { return __uint_as_float(v & 0xffff0000u); }
__device__ __forceinline__ float half_sum(float v) {
  auto r = __builtin_amdgcn_permlane32_swap(__float_as_uint(v), __float_as_uint(v), false, false);
  return __uint_as_float(r[0]) + __uint_as_float(r[1]);
}
__device__ __forceinline__ float half_max(float v) {
  auto r = __builtin_amdgcn_permlane32_swap(__float_as_uint(v), __float_as_uint(v), false, false);
  return __builtin_fmaxf(__uint_as_float(r[0]), __uint_as_float(r[1]));
}
__device__ __forceinline__ u32x4 pack8(const f32x16& v, int b) {
  return (u32x4){cvtpk(v[b], v[b + 1]), cvtpk(v[b + 2], v[b + 3]), cvtpk(v[b + 4], v[b + 5]), cvtpk(v[b + 6], v[b + 7])};
}
#define SB() __builtin_amdgcn_sched_barrier(0)
#define WAITV(n) asm volatile("s_waitcnt vmcnt(%0)" ::"i"(n) : "memory")
#define WAITV_BAR(n) asm volatile("s_waitcnt vmcnt(%0) lgkmcnt(0)\n\ts_barrier" ::"i"(n) : "memory")

#define STAMP(k, i)
#define TR(i)
constexpr int K1_SLOT = 28672, K1_NS = 5, K1_TAB = K1_NS * K1_SLOT, K1_LDS = K1_TAB + 2048;
#define K1ROT ((bi & 3) * 2)
__device__ __forceinline__ void phase_qkv(char* lds, const u16* __restrict__ x, const u16* __restrict__ wqkv, char* __restrict__ ws, int wid, int lane, int xcd, int bi) {
  const int w = wid & 3;
  const int panel = 4 * (xcd & 3) + (bi & 3), g = 8 * (xcd >> 2) + (bi >> 2);
  const int tt = 4 * panel + w;
  const int h = lane >> 5, tok = lane & 31;
  const lds_cptr L = (lds_cptr)lds;
  const int grp1 = (lane >> 4) & 1, q = (lane & 15) >> 2, p = lane & 3;
  const lds_cptr wb = L + 16384 + (8 * h + q) * 64 + grp1 * 32 + p * 8;
  unsigned xb_[3][4], wb_[3];
#pragma unroll
  for (int sp = 0; sp < 3; ++sp) {
    wb_[sp] = (unsigned)(uintptr_t)wb + sp * 2 * K1_SLOT; asm volatile("" : "+v"(wb_[sp]));
#pragma unroll
    for (int ks = 0; ks < 4; ++ks) { xb_[sp][ks] = (unsigned)(uintptr_t)L + (32 * w + tok) * 128 + (((2 * ks + h) ^ ((tok >> 1) & 7)) * 16) + sp * 2 * K1_SLOT; asm volatile("" : "+v"(xb_[sp][ks])); }
  }
#define K1_XP(s, ks) ((lds_cptr)(uintptr_t)xb_[((s) % K1_NS) >> 1][ks] + (((s) % K1_NS) & 1) * K1_SLOT)
#define K1_WP(s) ((lds_cptr)(uintptr_t)wb_[((s) % K1_NS) >> 1] + (((s) % K1_NS) & 1) * K1_SLOT)
  LAS float* T = (LAS float*)(lds + K1_TAB);
  LAS float* cs = T;
  LAS float* st = T + 96 + w * 64;
  STAMP(0, 0);
  if (wid >= 8) {
    if (w == 0 && lane < 28) *(LAS unsigned*)(lds + F_CNT + lane * 4) = 0;
    const unsigned lds0 = (unsigned)(uintptr_t)lds;
    const srd_t sx = SRD(x, SEQ * DM * 2), sw = SRD(wqkv, DM * 3 * DM * 2);
    const unsigned vW = (lane >> 2) * 3072 + (lane & 3) * 16;
    const unsigned vXa = (lane >> 3) * 1024 + (((lane & 7) ^ (lane >> 4)) * 16);
    const unsigned vXb = (lane >> 3) * 1024 + (((lane & 7) ^ (4 + (lane >> 4))) * 16);
    auto issue = [&](int s) {
      const unsigned sl = lds0 + (s % K1_NS) * K1_SLOT;
      const int sk = (s + (K1ROT)) & 7;
#pragma unroll
      for (int i = 0; i < 3; ++i) {
        const int idx = 3 * w + i, tile = idx >> 2, part = idx & 3;
        dma16(sl + 16384 + tile * 4096 + part * 1024, vW, sw, (64 * sk + 16 * part) * 3072 + tile * 1024 + g * 64);
      }
#pragma unroll
      for (int i = 0; i < 4; ++i) dma16(sl + (32 * w + 8 * i) * 128, (i & 1) ? vXb : vXa, sx, (tt * 32 + 8 * i) * 1024 + sk * 128);
    };
    asm volatile("s_nop 4");
    issue(0); issue(1);
#pragma unroll
    for (int s = 0; s < 8; ++s) {
      if (s == 0) WAITV_BAR(7); else if (s <= 5) WAITV_BAR(14); else if (s == 6) WAITV_BAR(7); else WAITV_BAR(0);
      if (s == 0) issue(2);
      if (s + 3 < 8) issue(s + 3);
    }
    asm volatile("s_barrier" ::: "memory");
  } else if (wid >= 4) {
    const u32x4* xrow = (const u32x4*)(x + (size_t)(tt * 32 + tok) * DM + g * 32 + h * 8);
    const u32x4 xs0 = xrow[0], xs1 = xrow[2];
    u64 tag = 0;
    if (w == 0) tag = __hip_atomic_load((gu64*)(ws + WS_SYNC), __ATOMIC_RELAXED, __HIP_MEMORY_SCOPE_AGENT) + 1;
    f32x4 s1[2] = {}, s2[2] = {}, cc[2] = {};
    const bf16x8 ones = {0x3f80, 0x3f80, 0x3f80, 0x3f80, 0x3f80, 0x3f80, 0x3f80, 0x3f80};
    const int i16 = lane & 15, g4 = lane >> 4;
    unsigned xa_[3][4], wa_[3];
#pragma unroll
    for (int sp = 0; sp < 3; ++sp) {
#pragma unroll
      for (int u = 0; u < 4; ++u) { const int row = 32 * w + 16 * (u >> 1) + i16, c = 4 * (u & 1) + g4;
        xa_[sp][u] = (unsigned)(uintptr_t)L + row * 128 + ((c ^ ((row >> 1) & 7)) * 16) + sp * 2 * K1_SLOT; asm volatile("" : "+v"(xa_[sp][u])); }
      wa_[sp] = (unsigned)(uintptr_t)L + 16384 + (w < 3 ? w : 2) * 4096 + (8 * g4 + q) * 64 + p * 8 + sp * 2 * K1_SLOT; asm volatile("" : "+v"(wa_[sp]));
    }
    bf16x8 xa[2][4], wa[2][4];
#pragma unroll
    for (int s = 0; s < 9; ++s) {
      if (s < 8) {
        asm volatile("s_barrier" ::: "memory");
        const int sp = (s % K1_NS) >> 1, so = ((s % K1_NS) & 1) * K1_SLOT;
#pragma unroll
        for (int u = 0; u < 4; ++u) {
          xa[s & 1][u] = *(LAS const bf16x8*)((lds_cptr)(uintptr_t)xa_[sp][u] + so);
          wa[s & 1][u] = tr_frag((lds_cptr)(uintptr_t)wa_[sp] + so + (u >> 1) * 32 + (u & 1) * 2048, 256);
        }
      }
      __builtin_amdgcn_sched_barrier(0);
      if (s > 0) {
        const int b = (s - 1) & 1;
#pragma unroll
        for (int u = 0; u < 4; ++u) {
          s2[u >> 1] = __builtin_amdgcn_mfma_f32_16x16x32_bf16(xa[b][u], xa[b][u], s2[u >> 1], 0, 0, 0);
          s1[u >> 1] = __builtin_amdgcn_mfma_f32_16x16x32_bf16(xa[b][u], ones, s1[u >> 1], 0, 0, 0);
          cc[u >> 1] = __builtin_amdgcn_mfma_f32_16x16x32_bf16(wa[b][u], ones, cc[u >> 1], 0, 0, 0);
        }
      }
      __builtin_amdgcn_sched_barrier(0);
    }
    {
      const int r = i16 & 3;
      const bool diag = (i16 >> 2) == g4;
#pragma unroll
      for (int th = 0; th < 2; ++th) {
        const float a1 = r == 0 ? s1[th][0] : r == 1 ? s1[th][1] : r == 2 ? s1[th][2] : s1[th][3];
        const float a2 = r == 0 ? s2[th][0] : r == 1 ? s2[th][1] : r == 2 ? s2[th][2] : s2[th][3];
        const float mean = a1 * (1.0f / DM), var = __builtin_fmaxf(a2 * (1.0f / DM) - mean * mean, 0.f);
        const float rs = __builtin_amdgcn_rsqf(var + 1e-5f);
        if (diag) { st[16 * th + i16] = rs; st[32 + 16 * th + i16] = -mean * rs; }
        if (w < 3 && i16 == 0) *(LAS f32x4*)(cs + w * 32 + 16 * th + 4 * g4) = cc[th];
      }
    }
    if (w == 0 && lane == 0) *(LAS u64*)(lds + TAG_W) = tag;
    asm volatile("s_waitcnt lgkmcnt(0)\n\ts_barrier" ::: "memory");
    const float rstd = st[tok], nm = st[32 + tok];
    const srd_t sXi = SRD(ws + WS_XN, IMG_BYTES);
    u32x4 y;
#pragma unroll
    for (int r = 0; r < 4; ++r) y[r] = cvtpk(bflo(xs0[r]) * rstd + nm, bfhi(xs0[r]) * rstd + nm);
    store16<16>(sXi, lane * 16, (tt * 32 + 2 * g) * 1024, y);
#pragma unroll
    for (int r = 0; r < 4; ++r) y[r] = cvtpk(bflo(xs1[r]) * rstd + nm, bfhi(xs1[r]) * rstd + nm);
    store16<16>(sXi, lane * 16, (tt * 32 + 2 * g + 1) * 1024, y);
  } else {
    f32x16 aq = {}, ak = {}, av = {};
    bf16x8 xf[3], wq[3], wk[3], wv[3];
#pragma unroll
    for (int n = 0; n < 34; ++n) {
      const int s = n >> 2, ks = n & 3, c = n % 3, m = (n + 1) % 3;
      if (n < 32 && ks == 0) { asm volatile("s_barrier" ::: "memory"); if (s == 0) STAMP(0, 1); }
      SB();
      if (n >= 2) aq = MFMA(wq[m], xf[m], aq);
      SB();
      if (n < 32) { xf[c] = *(LAS const bf16x8*)K1_XP(s, ks); wq[c] = tr_frag(K1_WP(s) + ks * 1024, 256); }
      SB();
      if (n >= 2) ak = MFMA(wk[m], xf[m], ak);
      SB();
      if (n < 32) wk[c] = tr_frag(K1_WP(s) + 4096 + ks * 1024, 256);
      SB();
      if (n >= 2) av = MFMA(xf[m], wv[m], av);
      SB();
      if (n < 32) wv[c] = tr_frag(K1_WP(s) + 8192 + ks * 1024, 256);
      SB();
    }
    STAMP(0, 2);
    asm volatile("s_barrier" ::: "memory");
    const float rstd = st[tok], nm = st[32 + tok], cv = cs[64 + tok];
    f32x4 cq4[4], ck4[4], r4[4], m4[4];
#pragma unroll
    for (int rg = 0; rg < 4; ++rg) { cq4[rg] = *(LAS const f32x4*)(cs + 8 * rg + 4 * h); ck4[rg] = *(LAS const f32x4*)(cs + 32 + 8 * rg + 4 * h);
      r4[rg] = *(LAS const f32x4*)(st + 8 * rg + 4 * h); m4[rg] = *(LAS const f32x4*)(st + 32 + 8 * rg + 4 * h); }
    const float QS = 0.25f * 1.4426950408889634f;
    const srd_t sQi = SRD(ws + WS_Q, IMG_BYTES), sKi = SRD(ws + WS_K, IMG_BYTES), sVi = SRD(ws + WS_V, IMG_BYTES);
    const unsigned so = ((2 * g) * 64 + tt) * 1024;
    f32x16 o;
    const float rq = rstd * QS, nq = nm * QS;
#pragma unroll
    for (int r = 0; r < 16; ++r) o[r] = ak[r] * rstd + nm * ck4[r >> 2][r & 3];
    store16<16>(sKi, lane * 16, so, pack8(o, 0));
    store16<16>(sKi, lane * 16, so + 65536, pack8(o, 8));
#pragma unroll
    for (int r = 0; r < 16; ++r) o[r] = av[r] * r4[r >> 2][r & 3] + m4[r >> 2][r & 3] * cv;
    const unsigned vv = grp1 * 65536 + h * 256 + (lane & 15) * 16;
    store16<16>(sVi, vv, so, pack8(o, 0));
    store16<16>(sVi, vv, so + 512, pack8(o, 8));
#pragma unroll
    for (int r = 0; r < 16; ++r) o[r] = aq[r] * rq + nq * cq4[r >> 2][r & 3];
    store16<16>(sQi, lane * 16, so, pack8(o, 0));
    store16<16>(sQi, lane * 16, so + 65536, pack8(o, 8));
  }
  asm volatile("s_waitcnt vmcnt(0) lgkmcnt(0)\n\ts_barrier" ::: "memory");
  if (wid == 4 && lane == 0) flag_store(ws + WS_FQ + (g * 16 + panel) * 8, *(LAS const u64*)(lds + TAG_W));
  STAMP(0, 3);
}

constexpr float REF_OFF = 88.f;
#define ATT_UNROLL 8
#define STR2(x) #x
#define STR(x) STR2(x)
#define ATT_CLOB "v64","v65","v66","v67","v68","v69","v70","v71","v72","v73","v74","v75","v76","v77","v78","v79", \
  "v80","v81","v82","v83","v84","v85","v86","v87","v88","v89","v90","v91","v92","v93","v94","v95", \
  "v96","v97","v98","v99","v100","v101","v102","v103","v104","v105","v106","v107", \
  "v108","v109","v110","v111","v112","v113","v114","v115","v116","v117","v118","v119","v120","v121","v122","v123"
__device__ __forceinline__ void attn_fast(f32x16& o, const f32x16& c, const bf16x8& qf, unsigned kaddr, unsigned vaddr, unsigned cntaddr,
                                          srd_t sk, srd_t sv, unsigned voff, unsigned sL, unsigned sG) {
  const unsigned long long vmask = 0x0000ffff0000ffffull;
  const unsigned onev = (threadIdx.x & 15) == 0 ? 0x3f803f80u : 0u;
  asm volatile(
      ".macro ATT_DMA I\n"
      "s_add_u32 m0, %[sl], (((\\I)&1)*65536 + (2+((\\I)>>1))*8192)\n"
      "s_add_u32 s41, %[sg], ((2+((\\I)>>1))*8192)\n"
      ".if ((\\I)&1)\n"
      "buffer_load_dwordx4 %[vo], %[sv], s41 offen sc1 lds\n"
      ".else\n"
      "buffer_load_dwordx4 %[vo], %[sk], s41 offen sc1 lds\n"
      ".endif\n"
      ".endm\n"
      ".macro ATT_TILE X, Y, VC, VN, T, D\n"
      "s_waitcnt lgkmcnt(0)\n"
      "v_exp_f32 v[\\X+0], v[\\X+0]\n"
      "v_exp_f32 v[\\X+1], v[\\X+1]\n"
      "v_cvt_pk_bf16_f32 v102, v[\\Y+12], v[\\Y+13]\n"
      "v_exp_f32 v[\\X+2], v[\\X+2]\n"
      "v_cvt_pk_bf16_f32 v103, v[\\Y+14], v[\\Y+15]\n"
      "v_mfma_f32_32x32x16_bf16 v[\\Y:\\Y+15], v[104:107], %[q], %[c]\n"
      "ds_read_b128 v[104:107], %[ka] offset:(\\T+2)*1024\n"
      "v_exp_f32 v[\\X+3], v[\\X+3]\n"
      "v_cvt_pk_bf16_f32 v96, v[\\X+0], v[\\X+1]\n"
      "v_exp_f32 v[\\X+4], v[\\X+4]\n"
      "v_exp_f32 v[\\X+5], v[\\X+5]\n"
      "v_mfma_f32_32x32x16_bf16 %[o], v[\\VN+4:\\VN+7], v[100:103], %[o]\n"
      "s_mov_b64 exec, %[m]\n"
      "ds_read_b128 v[\\VN:\\VN+3], %[va] offset:(\\T+1)*1024\n"
      "ds_read_b128 v[\\VN+4:\\VN+7], %[va] offset:(\\T+1)*1024+512\n"
      "s_mov_b64 exec, -1\n"
      "v_cvt_pk_bf16_f32 v97, v[\\X+2], v[\\X+3]\n"
      "v_exp_f32 v[\\X+6], v[\\X+6]\n"
      ".if (\\D) >= 0\n ATT_DMA (\\D)\n .endif\n"
      "v_exp_f32 v[\\X+7], v[\\X+7]\n"
      "v_cvt_pk_bf16_f32 v98, v[\\X+4], v[\\X+5]\n"
      "v_exp_f32 v[\\X+8], v[\\X+8]\n"
      "v_exp_f32 v[\\X+9], v[\\X+9]\n"
      ".if (\\D) >= 0\n ATT_DMA (\\D)+1\n .endif\n"
      "v_cvt_pk_bf16_f32 v99, v[\\X+6], v[\\X+7]\n"
      "v_exp_f32 v[\\X+10], v[\\X+10]\n"
      "v_exp_f32 v[\\X+11], v[\\X+11]\n"
      "v_cvt_pk_bf16_f32 v100, v[\\X+8], v[\\X+9]\n"
      "v_mfma_f32_32x32x16_bf16 %[o], v[\\VC:\\VC+3], v[96:99], %[o]\n"
      "v_exp_f32 v[\\X+12], v[\\X+12]\n"
      "v_exp_f32 v[\\X+13], v[\\X+13]\n"
      ".if (\\D) >= 0\n ATT_DMA (\\D)+2\n .endif\n"
      "v_cvt_pk_bf16_f32 v101, v[\\X+10], v[\\X+11]\n"
      "v_exp_f32 v[\\X+14], v[\\X+14]\n"
      "v_exp_f32 v[\\X+15], v[\\X+15]\n"
      ".endm\n"
      ".set att_i, 108\n"
      ".rept 16\n"
      "v_mov_b32 v[att_i], %[one]\n"
      ".set att_i, att_i+1\n"
      ".endr\n"
      "v_mov_b32 v100, 0\n v_mov_b32 v101, 0\n v_mov_b32 v92, 0\n v_mov_b32 v93, 0\n v_mov_b32 v94, 0\n v_mov_b32 v95, 0\n"
      "ds_read_b128 v[104:107], %[ka]\n"
      "s_mov_b64 exec, %[m]\n"
      "ds_read_b128 v[108:111], %[va]\n"
      "ds_read_b128 v[112:115], %[va] offset:512\n"
      "s_mov_b64 exec, -1\n"
      "s_waitcnt lgkmcnt(2)\n"
      "v_mfma_f32_32x32x16_bf16 v[64:79], v[104:107], %[q], %[c]\n"
      "ds_read_b128 v[104:107], %[ka] offset:1024\n"
      "s_nop 7\n s_nop 7\n"
      "ATT_TILE 64, 80, 108, 116, 0, 0\n"
      "ATT_TILE 80, 64, 116, 108, 1, 3\n"
      "ATT_TILE 64, 80, 108, 116, 2, 6\n"
      "ATT_TILE 80, 64, 116, 108, 3, 9\n"
      "ATT_TILE 64, 80, 108, 116, 4, -1\n"
      "ATT_TILE 80, 64, 116, 108, 5, -1\n"
      "ATT_TILE 64, 80, 108, 116, 6, -1\n"
      "ATT_TILE 80, 64, 116, 108, 7, -1\n"
      "v_add_u32 %[ka], 8192, %[ka]\n"
      "v_add_u32 %[va], 8192, %[va]\n"
      "s_waitcnt vmcnt(0)\n"
      "v_mov_b32 v124, 1\n"
      "s_mov_b64 exec, 1\n"
      "ds_add_u32 %[cn], v124\n"
      "s_mov_b64 exec, -1\n"
      "s_mov_b32 s41, 0\n"
      "att_poll%=:\n"
      "ds_read_b32 v124, %[cn]\n"
      "s_add_u32 s41, s41, 1\n"
      "s_cmp_gt_u32 s41, 0x40000\n"
      "s_cbranch_scc1 att_go%=\n"
      "s_waitcnt lgkmcnt(0)\n"
      "v_readfirstlane_b32 s42, v124\n"
      "s_nop 0\n"
      "s_cmp_ge_u32 s42, 8\n"
      "s_cbranch_scc0 att_poll%=\n"
      "att_go%=:\n"
      "s_movk_i32 s40, 7\n"
      "att_loop%=:\n"
      ".set att_t, 0\n"
      ".rept 4\n"
      "ATT_TILE 64, 80, 108, 116, att_t, -1\n"
      "ATT_TILE 80, 64, 116, 108, att_t+1, -1\n"
      ".set att_t, att_t+2\n"
      ".endr\n"
      "v_add_u32 %[ka], 8192, %[ka]\n"
      "v_add_u32 %[va], 8192, %[va]\n"
      "s_sub_u32 s40, s40, 1\n"
      "s_cmp_lg_u32 s40, 0\n"
      "s_cbranch_scc1 att_loop%=\n"
      "s_waitcnt lgkmcnt(0)\n"
      "v_cvt_pk_bf16_f32 v102, v92, v93\n"
      "v_cvt_pk_bf16_f32 v103, v94, v95\n"
      "s_nop 1\n"
      "v_mfma_f32_32x32x16_bf16 %[o], v[120:123], v[100:103], %[o]\n"
      "s_nop 7\n s_nop 7\n"
      ".purgem ATT_TILE\n"
      ".purgem ATT_DMA\n"
      : [o] "+v"(o), [ka] "+v"(kaddr), [va] "+v"(vaddr)
      : [c] "v"(c), [q] "v"(qf), [m] "s"(vmask), [one] "v"(onev), [cn] "v"(cntaddr), [sk] "s"(sk), [sv] "s"(sv), [vo] "v"(voff), [sl] "s"(sL), [sg] "s"(sG)
      : "memory", "s40", "s41", "s42", "scc", "v124", ATT_CLOB);
}

__device__ __forceinline__ void attn_safe(f32x16& o, bf16x8 qf, lds_cptr kp, lds_cptr vp, int lane) {
  o = f32x16{};
  float m = -INFINITY;
  const bf16x8 ones = {0x3f80, 0x3f80, 0x3f80, 0x3f80, 0x3f80, 0x3f80, 0x3f80, 0x3f80};
#pragma unroll 1
  for (int t = 0; t < NTT; ++t) {
    const bf16x8 kf = *(LAS const bf16x8*)(kp + t * 1024);
    f32x16 s = MFMA(kf, qf, f32x16{});
    float mx = s[0];
#pragma unroll
    for (int r = 1; r < 16; ++r) mx = __builtin_fmaxf(mx, s[r]);
    mx = __builtin_fmaxf(half_max(mx), m);
    const float a = __builtin_amdgcn_exp2f(m - mx);
    m = mx;
#pragma unroll
    for (int r = 0; r < 16; ++r) { o[r] *= a; s[r] = __builtin_amdgcn_exp2f(s[r] - m); }
    bf16x8 v0 = *(LAS const bf16x8*)(vp + t * 1024), v1 = *(LAS const bf16x8*)(vp + t * 1024 + 512);
    if (lane & 16) { v0 = ones; v1 = ones; }
    o = MFMA(v0, __builtin_bit_cast(bf16x8, pack8(s, 0)), o);
    o = MFMA(v1, __builtin_bit_cast(bf16x8, pack8(s, 8)), o);
  }
}

__device__ __forceinline__ u32x4 attn_wave(const char* lds, bf16x8 qf, int lane, unsigned cntaddr, srd_t sk, srd_t sv, unsigned sL, unsigned sG) {
  const lds_cptr kp = (lds_cptr)lds + lane * 16;
  const lds_cptr vp = (lds_cptr)lds + 65536 + (lane >> 5) * 256 + (lane & 15) * 16;
  float mx;
  {
    const f32x16 s0 = MFMA(*(LAS const bf16x8*)(kp), qf, f32x16{}), s1 = MFMA(*(LAS const bf16x8*)(kp + 1024), qf, f32x16{});
    mx = __builtin_fmaxf(s0[0], s1[0]);
#pragma unroll
    for (int r = 1; r < 16; ++r) mx = __builtin_fmaxf(__builtin_fmaxf(mx, s0[r]), s1[r]);
    mx = half_max(mx);
  }
  const float nref = -(mx + REF_OFF);
  f32x16 c, o = {};
#pragma unroll
  for (int r = 0; r < 16; ++r) c[r] = nref;
  attn_fast(o, c, qf, (unsigned)(uintptr_t)kp, (unsigned)(uintptr_t)vp, cntaddr, sk, sv, lane * 16, sL, sG);
  STAMP(1, 2);
  { auto r = __builtin_amdgcn_permlane32_swap(__float_as_uint(o[8]), __float_as_uint(o[8]), false, false); o[8] = __uint_as_float(r[0]); }
  float amax = 0.f; bool bad = !(o[8] < 0x1p126f);
#pragma unroll
  for (int r = 0; r < 8; ++r) { const float a = __builtin_fabsf(o[r]); bad |= !(a < 0x1p126f); amax = __builtin_fmaxf(amax, a); }
  bad |= !(amax > 0x1p-100f);
  if (__builtin_expect(__any(bad), 0)) attn_safe(o, qf, kp, vp, lane);
  const float rl = __builtin_amdgcn_rcpf(o[8]);
#pragma unroll
  for (int r = 0; r < 8; ++r) o[r] *= rl;
  return pack8(o, 0);
}

__device__ __forceinline__ float nopk(float v) { asm("" : "+v"(v)); return v; }
__device__ __forceinline__ float silu(float v) { return v * __builtin_amdgcn_rcpf(1.0f + __builtin_amdgcn_exp2f(-1.4426950408889634f * v)); }
#define FPRIO 2
#define POLLLO 1
#define POLL_PRIO_LO "s_setprio 0\n"
#define POLL_PRIO_HI "s_setprio " STR(FPRIO) "\n"
__device__ __forceinline__ void fbar(unsigned cntaddr, unsigned target) {
  unsigned t, sv, sp;
  asm volatile(
      "v_mov_b32 %0, 1\n"
      "s_mov_b64 exec, 1\n"
      "ds_add_u32 %3, %0\n"
      "s_mov_b64 exec, -1\n"
      "s_mov_b32 %2, 0\n"
      POLL_PRIO_LO
      "fb_poll%=:\n"
      "ds_read_b32 %0, %3\n"
      "s_add_u32 %2, %2, 1\n"
      "s_cmp_gt_u32 %2, 0x40000\n"
      "s_cbranch_scc1 fb_done%=\n"
      "s_waitcnt lgkmcnt(0)\n"
      "v_readfirstlane_b32 %1, %0\n"
      "s_nop 0\n"
      "s_cmp_ge_u32 %1, %4\n"
      "s_cbranch_scc0 fb_poll%=\n"
      "fb_done%=:\n"
      POLL_PRIO_HI
      "s_waitcnt lgkmcnt(0)\n"
      : "=&v"(t), "=&s"(sv), "=&s"(sp) : "v"(cntaddr), "s"(target) : "memory", "scc");
}
__device__ __forceinline__ void f_arrive(unsigned cntaddr) {
  unsigned t;
  asm volatile("v_mov_b32 %0, 1\n\ts_mov_b64 exec, 1\n\tds_add_u32 %1, %0\n\ts_mov_b64 exec, -1" : "=&v"(t) : "v"(cntaddr) : "memory");
}
__device__ __forceinline__ void f_check(unsigned cntaddr, unsigned target) {
  unsigned t, sv, sp;
  asm volatile(
      "s_mov_b32 %2, 0\n"
      "fc_poll%=:\n"
      "ds_read_b32 %0, %3\n"
      "s_add_u32 %2, %2, 1\n"
      "s_cmp_gt_u32 %2, 0x40000\n"
      "s_cbranch_scc1 fc_done%=\n"
      "s_waitcnt lgkmcnt(0)\n"
      "v_readfirstlane_b32 %1, %0\n"
      "s_nop 0\n"
      "s_cmp_ge_u32 %1, %4\n"
      "s_cbranch_scc0 fc_poll%=\n"
      "fc_done%=:\n"
      "s_waitcnt lgkmcnt(0)\n"
      : "=&v"(t), "=&s"(sv), "=&s"(sp) : "v"(cntaddr), "s"(target) : "memory", "scc");
}
__device__ __forceinline__ void dma16_48(unsigned lds_base, unsigned voff, srd_t srd, unsigned soff) {
  const unsigned long long m48 = 0xffffffffffffull;
  asm volatile("s_mov_b32 m0, %0\n\ts_mov_b64 exec, %4\n\tbuffer_load_dwordx4 %1, %2, %3 offen lds\n\ts_mov_b64 exec, -1" ::"s"(lds_base), "v"(voff), "s"(srd), "s"(soff), "s"(m48) : "memory");
}
#define FSLEEP 1
#define DSLEEP 3
#define SLP(n) do { SB(); __builtin_amdgcn_s_sleep(n); SB(); } while (0)
#define FPACE() SLP(1)
#define FPACE_A() SLP(1)
#define DPACE() SLP(1)
#define DPACE_A()
__device__ __forceinline__ void ffn_up(char* lds, const u16* w1, const u16* w3, char* ws, int panel, int gi, int w, int lane, u64 tag) {
  const unsigned lds0 = (unsigned)(uintptr_t)lds;
  const int tt0 = 8 * panel + 2 * w, h = lane >> 5;
  const int ntile = gi < 22 ? 3 : 2, t0 = gi < 22 ? 3 * gi : 66 + 2 * (gi - 22);
  const srd_t s1 = SRD(w1, DM * FF * 2), s3 = SRD(w3, DM * FF * 2), sx = SRD(ws + WS_XN, IMG_BYTES), sh = SRD(ws + WS_H, H_BYTES);
  const unsigned vw = (lane / 6) * (FF * 2) + (lane % 6) * 16, vx = lane * 16;
  auto issueW = [&](int j) {
    const unsigned so = (32 * j + 8 * w) * (FF * 2) + t0 * 32, dst = lds0 + F_BASE + (j % F_NS) * F_SLOT + w * 768;
    dma16_48(dst, vw, s1, so);
    dma16_48(dst + 3200, vw, s3, so);
  };
  u32x4 xr[2][4];
  auto issueX = [&](int j) {
    const unsigned so = (tt0 * 32 + 2 * j) * 1024;
    asm volatile("buffer_load_dwordx4 %0, %4, %5, %6 offen sc1\n\tbuffer_load_dwordx4 %1, %4, %5, %7 offen sc1\n\t"
                 "buffer_load_dwordx4 %2, %4, %5, %6 offen offset:1024 sc1\n\tbuffer_load_dwordx4 %3, %4, %5, %7 offen offset:1024 sc1"
                 : "=&v"(xr[j & 1][0]), "=&v"(xr[j & 1][1]), "=&v"(xr[j & 1][2]), "=&v"(xr[j & 1][3]) : "v"(vx), "s"(sx), "s"(so), "s"(so + 32768) : "memory");
  };
  const int grp1 = (lane >> 4) & 1, q = (lane & 15) >> 2, p = lane & 3;
  const lds_cptr wb = (lds_cptr)lds + F_BASE + (grp1 ? 3200 : 0) + (8 * h + q) * 96 + 8 * p;
  asm volatile("s_nop 4");
  issueW(0); issueW(1); issueW(2);
  if (w == 0) flags_wait(ws + WS_FQ + ((lane >> 1) * 16 + 2 * panel + (lane & 1)) * 8, lane < 32, tag);
  asm volatile("s_barrier" ::: "memory");
  issueX(0); issueX(1);
  asm volatile("s_barrier" ::: "memory");
  f32x16 acc[2][3] = {};
  bf16x8 wf[2][3];
  const unsigned cu = lds0 + CNT_UP;
#define XWAIT(n, b) asm volatile("s_waitcnt vmcnt(%4) lgkmcnt(0)" : "+v"(xr[b][0]), "+v"(xr[b][1]), "+v"(xr[b][2]), "+v"(xr[b][3]) : "i"(n) : "memory")
  XWAIT(4, 0);
  f_arrive(cu); f_arrive(cu);
  f_check(cu, 8);
#pragma unroll
  for (int T = 0; T < 3; ++T) wf[0][T] = tr_frag(wb + T * 32, 384);
#pragma unroll
  for (int j = 0; j < 16; ++j) {
    if (j == 1) XWAIT(6, 1);
    else if (j >= 2) XWAIT((j + 2 <= 15 ? 2 : 0) + (j + 1 <= 15 ? 4 : 0), j & 1);
    if (j >= 1 && j + 1 <= 15) f_arrive(cu);
    SB();
#pragma unroll
    for (int T = 0; T < 3; ++T) {
      acc[0][T] = MFMA(wf[0][T], __builtin_bit_cast(bf16x8, xr[j & 1][0]), acc[0][T]);
      SB();
      wf[1][T] = tr_frag(wb + (j % F_NS) * F_SLOT + 1536 + T * 32, 384);
      FPACE_A();
      acc[1][T] = MFMA(wf[0][T], __builtin_bit_cast(bf16x8, xr[j & 1][1]), acc[1][T]);
      if (T < 2) FPACE();
    }
    SB();
    if (j < 15) {
      if (j >= 1) f_check(cu, 4 * (j + 2));
      if (j + 3 < 16) issueW(j + 3);
    }
    SB();
#pragma unroll
    for (int T = 0; T < 3; ++T) {
      acc[0][T] = MFMA(wf[1][T], __builtin_bit_cast(bf16x8, xr[j & 1][2]), acc[0][T]);
      FPACE_A();
      acc[1][T] = MFMA(wf[1][T], __builtin_bit_cast(bf16x8, xr[j & 1][3]), acc[1][T]);
      SB();
      if (j < 15) wf[0][T] = tr_frag(wb + ((j + 1) % F_NS) * F_SLOT + T * 32, 384);
      if (T < 2) FPACE();
    }
    SB();
    if (j + 2 < 16) issueX(j + 2);
    SB();
  }
#pragma unroll
  for (int T = 0; T < 3; ++T)
#pragma unroll
    for (int th = 0; th < 2; ++th) {
      f32x16 hv;
#pragma unroll
      for (int r = 0; r < 8; ++r) hv[r] = nopk(silu(acc[th][T][r]) * acc[th][T][8 + r]);
      if (T < ntile) store16<16>(sh, lane * 16, ((tt0 + th) * NFG + t0 + T) * 1024, pack8(hv, 0));
    }
}

__device__ __forceinline__ void lds_wait(unsigned addr, unsigned want) {
  for (unsigned spins = 0; spins < (1u << 18); ++spins) {
    unsigned v; asm volatile("ds_read_b32 %0, %1\n\ts_waitcnt lgkmcnt(0)" : "=&v"(v) : "v"(addr) : "memory");
    if ((unsigned)__builtin_amdgcn_readfirstlane(v) >= want) break;
    __builtin_amdgcn_s_sleep(4);
  }
}

constexpr int DN_NS = 3, DN_HD = 5;
__device__ __forceinline__ void ffn_down(char* lds, const u16* w2, char* ws, int tb, int nb, int w, int lane, f32x16& p00, f32x16& p01, f32x16& p10, f32x16& p11) {
  const unsigned lbase = (unsigned)(uintptr_t)lds, lds0 = lbase + F_BASE + w * (DN_NS * 2048);
  const int h = lane >> 5;
  const int k0 = w < 2 ? 22 * w : 44 + 21 * (w - 2), nst = w < 2 ? 22 : 21;
  const srd_t s2 = SRD(w2, FF * DM * 2), sh = SRD(ws + WS_H, H_BYTES);
  const unsigned vw = (lane >> 3) * 1024 + (((lane & 7) ^ (((lane >> 4) & 1) << 2)) * 16), vh = lane * 16;
  const int grp1 = (lane >> 4) & 1, q = (lane & 15) >> 2, p = lane & 3;
  const lds_cptr L = (lds_cptr)lds + F_BASE + w * (DN_NS * 2048);
  const lds_cptr b0 = L + (4 * h + q) * 128 + ((q >> 1) & 1) * 64 + grp1 * 32 + p * 8;
  const lds_cptr b1 = L + (4 * h + q) * 128 + (((q >> 1) & 1) ^ 1) * 64 + grp1 * 32 + p * 8;
  u32x4 hf[DN_HD][2];
  auto issueW = [&](int j) {
    const unsigned so = (k0 + (j < nst ? j : 0)) * 16 * 1024 + nb * 128;
    dma16(lds0 + (j % DN_NS) * 2048, vw, s2, so);
    dma16(lds0 + (j % DN_NS) * 2048 + 1024, vw, s2, so + 8192);
  };
  auto issueH = [&](int j) {
    const unsigned ho = j < nst ? (2 * tb * NFG + k0 + j) * 1024 : 0x7ff00000u;
    asm volatile("buffer_load_dwordx4 %0, %2, %3, %4 offen sc1\n\tbuffer_load_dwordx4 %1, %2, %3, %5 offen sc1"
                 : "=&v"(hf[j % DN_HD][0]), "=&v"(hf[j % DN_HD][1]) : "v"(vh), "s"(sh), "s"(ho), "s"(ho + (j < nst ? NFG * 1024 : 0)) : "memory");
  };
  asm volatile("s_nop 4");
  issueH(0); issueH(1); issueW(0); issueH(2); issueW(1); issueH(3); issueW(2);
  f32x16 a00 = {}, a01 = {}, a10 = {}, a11 = {};
  bf16x8 w0[2], w1f[2];
  asm volatile("s_waitcnt vmcnt(8)" : "+v"(hf[0][0]), "+v"(hf[0][1])::"memory");
  w0[0] = tr_frag(b0, 1024); w1f[0] = tr_frag(b1, 1024);
#pragma unroll
  for (int j = 0; j < 22; ++j) {
    const int c = j & 1, n = c ^ 1;
    if (j + 1 < 22) {
      const int young = j < 2 ? 4 : (j + 2 < 22 ? 4 : 0) + (j + 3 < 22 ? 2 : 0);
      asm volatile("s_waitcnt vmcnt(%2) lgkmcnt(0)" : "+v"(hf[(j + 1) % DN_HD][0]), "+v"(hf[(j + 1) % DN_HD][1]) : "i"(young) : "memory");
    } else asm volatile("s_waitcnt lgkmcnt(0)" ::: "memory");
    if (j + 3 < 22) issueW(j + 3);
    if (j + 4 < 22) issueH(j + 4);
    const bf16x8 h0 = __builtin_bit_cast(bf16x8, hf[j % DN_HD][0]), h1 = __builtin_bit_cast(bf16x8, hf[j % DN_HD][1]);
    SB();
    a00 = MFMA(w0[c], h0, a00);
    SB();
    if (j + 1 < 22) w0[n] = tr_frag(b0 + ((j + 1) % DN_NS) * 2048, 1024);
    DPACE_A();
    a01 = MFMA(w0[c], h1, a01);
    DPACE();
    a10 = MFMA(w1f[c], h0, a10);
    SB();
    if (j + 1 < 22) w1f[n] = tr_frag(b1 + ((j + 1) % DN_NS) * 2048, 1024);
    DPACE_A();
    a11 = MFMA(w1f[c], h1, a11);
    DPACE();
  }
  p00 = a00; p01 = a01; p10 = a10; p11 = a11;
}

__device__ __forceinline__ void final_tile(char* lds, const u16* x, const u16* wo, char* ws, u16* out, const f32x16& a00, const f32x16& a01, const f32x16& a10, const f32x16& a11,
                                           int panel, int tb, int nb, int w, int lane, unsigned& nbar, u64 tag) {
  const unsigned lds0 = (unsigned)(uintptr_t)lds, cnt = lds0 + CNT_F;
  const int nh = w >> 1, th = w & 1, tt = 2 * tb + th, h = lane >> 5;
  const srd_t so = SRD(wo, DM * DM * 2), sO = SRD(ws + WS_O, IMG_BYTES);
  const size_t ob = (size_t)(tt * 32 + (lane & 31)) * DM + nb * 64 + nh * 32 + 4 * h;
  u32x2 xv[4];
#pragma unroll
  for (int i = 0; i < 4; ++i) xv[i] = *(const u32x2*)(x + ob + 8 * i);
  lds_wait(lds0 + CNT_KV + 4, 8);
  const unsigned vw = (lane >> 3) * 1024 + (((lane & 7) ^ (((lane >> 4) & 1) << 2)) * 16);
#pragma unroll
  for (int i = 0; i < 16; ++i) dma16(lds0 + (4 * i + w) * 1024, vw, so, (4 * i + w) * 8192 + nb * 128);
  LAS f32x4* R = (LAS f32x4*)(lds + 65536);
#pragma unroll
  for (int qd = 0; qd < 4; ++qd) {
    const f32x16& snd = qd == 0 ? a00 : qd == 1 ? a01 : qd == 2 ? a10 : a11;
#pragma unroll
    for (int i = 0; i < 4; ++i) R[((qd * 4 + w) * 4 + i) * 64 + lane] = (f32x4){snd[4 * i], snd[4 * i + 1], snd[4 * i + 2], snd[4 * i + 3]};
  }
  asm volatile("s_waitcnt lgkmcnt(0)" ::: "memory");
  nbar += 4; fbar(cnt, nbar);
  f32x16 g;
#pragma unroll
  for (int i = 0; i < 4; ++i) {
    const f32x4 v0 = R[((w * 4 + 0) * 4 + i) * 64 + lane], v1 = R[((w * 4 + 1) * 4 + i) * 64 + lane], v2 = R[((w * 4 + 2) * 4 + i) * 64 + lane], v3 = R[((w * 4 + 3) * 4 + i) * 64 + lane];
#pragma unroll
    for (int e = 0; e < 4; ++e) g[4 * i + e] = silu((v0[e] + v1[e]) + (v2[e] + v3[e]));
  }
  const u32x4 g0 = pack8(g, 0), g1 = pack8(g, 8);
  if (w == 0) flags_wait(ws + WS_FO + (panel * 32 + (lane & 31)) * 8, lane < 32, tag);
  asm volatile("s_waitcnt vmcnt(0) lgkmcnt(0)" ::: "memory");
  nbar += 4; fbar(cnt, nbar);
  STAMP(2, 2);
  bf16x8 of[32];
#pragma unroll
  for (int hd = 0; hd < 32; ++hd) of[hd] = __builtin_bit_cast(bf16x8, __builtin_amdgcn_raw_buffer_load_b128(sO, lane * 16, (tt * 32 + hd) * 1024, 16));
  const int grp1 = (lane >> 4) & 1, q = (lane & 15) >> 2, p = lane & 3;
  const lds_cptr b = (lds_cptr)lds + (4 * h + q) * 128 + ((((q >> 1) & 1) ^ nh) * 64) + grp1 * 32 + p * 8;
  f32x16 a = {};
#pragma unroll
  for (int hd = 0; hd < 32; ++hd) a = MFMA(tr_frag(b + hd * 2048, 1024), of[hd], a);
#pragma unroll
  for (int i = 0; i < 4; ++i) {
    const unsigned ga = i < 2 ? g0[2 * i] : g1[2 * i - 4], gb = i < 2 ? g0[2 * i + 1] : g1[2 * i - 3];
    u32x2 y;
    y[0] = cvtpk(bflo(xv[i][0]) + a[4 * i] * bflo(ga), bfhi(xv[i][0]) + a[4 * i + 1] * bfhi(ga));
    y[1] = cvtpk(bflo(xv[i][1]) + a[4 * i + 2] * bflo(gb), bfhi(xv[i][1]) + a[4 * i + 3] * bfhi(gb));
    *(u32x2*)(out + ob + 8 * i) = y;
  }
}

constexpr int K_LDS = 163840;
__global__ __launch_bounds__(768) void recurrent_scan__mha_block__glu_gated_swiglu__prompt_kernel(
    const u16* __restrict__ x, const u16* __restrict__ wqkv, const u16* __restrict__ wo, const u16* __restrict__ w1, const u16* __restrict__ w3, const u16* __restrict__ w2,
    char* __restrict__ ws, u16* __restrict__ out) {
  extern __shared__ __attribute__((aligned(16))) char lds[];
  const int tid = threadIdx.x, lane = tid & 63;
  const int wid = __builtin_amdgcn_readfirstlane(tid >> 6);
  const int xcd = blockIdx.x & 7, bi = blockIdx.x >> 3;
  const unsigned lds0 = (unsigned)(uintptr_t)lds;
  phase_qkv(lds, x, wqkv, ws, wid, lane, xcd, bi);
  const u64 tag = *(LAS const u64*)(lds + TAG_W);
  if (wid < 8) {
    const int w = wid, hd = 4 * xcd + (bi >> 3), qb = bi & 7, tt = qb * 8 + w;
    const srd_t sk = SRD(ws + WS_K, IMG_BYTES), sv = SRD(ws + WS_V, IMG_BYTES), sq = SRD(ws + WS_Q, IMG_BYTES);
    STAMP(1, 0);
    if (w == 0) flags_wait(ws + WS_FQ + ((hd >> 1) * 16 + (lane & 15)) * 8, lane < 16, tag);
    asm volatile("s_barrier" ::: "memory");
    u32x4 qv;
    asm volatile("s_nop 4\n\tbuffer_load_dwordx4 %0, %1, %2, %3 offen sc1" : "=&v"(qv) : "v"(lane * 16), "s"(sq), "s"((hd * 64 + tt) * 1024) : "memory");
#pragma unroll
    for (int c = 0; c < 2; ++c) {
      dma16_sc1(lds0 + (c * 8 + w) * 1024, lane * 16, sk, hd * 65536 + (c * 8 + w) * 1024);
      dma16_sc1(lds0 + 65536 + (c * 8 + w) * 1024, lane * 16, sv, hd * 65536 + (c * 8 + w) * 1024);
    }
    asm volatile("s_waitcnt vmcnt(0)\n\ts_barrier" : "+v"(qv)::"memory");
    STAMP(1, 1);
    const u32x4 ov = attn_wave(lds, __builtin_bit_cast(bf16x8, qv), lane, lds0 + CNT_KV, sk, sv, lds0 + w * 1024, hd * 65536 + w * 1024);
    store16<16>(SRD(ws + WS_O, IMG_BYTES), lane * 16, (tt * 32 + hd) * 1024, ov);
    asm volatile("s_waitcnt vmcnt(0) lgkmcnt(0)" ::: "memory");
    unsigned old = 0;
    if (lane == 0) old = __hip_atomic_fetch_add((LAS unsigned*)(lds + CNT_KV + 4), 1u, __ATOMIC_RELAXED, __HIP_MEMORY_SCOPE_WORKGROUP);
    if (__builtin_amdgcn_readfirstlane(old) == 7 && lane == 0) flag_store(ws + WS_FO + (qb * 32 + hd) * 8, tag);
    STAMP(1, 3);
  } else {
    const int w = wid - 8;
    if (FPRIO) __builtin_amdgcn_s_setprio(FPRIO);
    unsigned nbar = 0;
    STAMP(2, 0);
    ffn_up(lds, w1, w3, ws, xcd, bi, w, lane, tag);
    asm volatile("s_waitcnt vmcnt(0) lgkmcnt(0)" ::: "memory");
    nbar += 4; fbar(lds0 + CNT_F, nbar);
    if (w == 0) {
      if (lane == 0) flag_store(ws + WS_FH + (xcd * 32 + bi) * 8, tag);
      flags_wait(ws + WS_FH + (xcd * 32 + (lane & 31)) * 8, lane < 32, tag);
    }
    nbar += 4; fbar(lds0 + CNT_F, nbar);
    STAMP(2, 1);
    const int tb = 4 * xcd + (bi >> 3), nb = bi & 7;
    f32x16 p00, p01, p10, p11;
    ffn_down(lds, w2, ws, tb, nb, w, lane, p00, p01, p10, p11);
    STAMP(3, 0);
    final_tile(lds, x, wo, ws, out, p00, p01, p10, p11, xcd, tb, nb, w, lane, nbar, tag);
    if (blockIdx.x == 0 && w == 0 && lane == 0) flag_store(ws + WS_SYNC, tag);
    STAMP(2, 3);
  }
}

extern "C" void kernel_launch(void* const* d_in, const int* in_sizes, int n_in, void* d_out, int out_size, void* d_ws, size_t ws_size, hipStream_t stream) {
  const u16* x = (const u16*)d_in[0]; const u16* wqkv = (const u16*)d_in[1]; const u16* wo = (const u16*)d_in[2];
  const u16* w1 = (const u16*)d_in[4]; const u16* w3 = (const u16*)d_in[5]; const u16* w2 = (const u16*)d_in[6];
  (void)hipFuncSetAttribute((const void*)recurrent_scan__mha_block__glu_gated_swiglu__prompt_kernel, hipFuncAttributeMaxDynamicSharedMemorySize, K_LDS);
  recurrent_scan__mha_block__glu_gated_swiglu__prompt_kernel<<<256, 768, K_LDS, stream>>>(x, wqkv, wo, w1, w3, w2, (char*)d_ws, (u16*)d_out);
}
```

```cpp
#include <hip/hip_runtime.h>
#include <hip/hip_bf16.h>
#include <stdint.h>

typedef unsigned short u16;
typedef short s16x4 __attribute__((ext_vector_type(4)));
typedef short bf16x8 __attribute__((ext_vector_type(8)));
typedef float f32x16 __attribute__((ext_vector_type(16)));
typedef float f32x4 __attribute__((ext_vector_type(4)));
typedef unsigned u32x4 __attribute__((ext_vector_type(4)));
typedef unsigned u32x2 __attribute__((ext_vector_type(2)));
typedef __bf16 bf16x2 __attribute__((ext_vector_type(2)));
#define LAS __attribute__((address_space(3)))
typedef LAS const char* lds_cptr;
typedef LAS char* lds_ptr;

constexpr int SEQ = 2048, DM = 512, NH = 32, FF = 1376;
constexpr int NTT = 64;
constexpr int NFG = 86;
constexpr size_t WS_Q = 0, WS_K = 2u << 20, WS_V = 4u << 20, WS_XN = 6u << 20, WS_O = 8u << 20, WS_G = 10u << 20, WS_H = 12u << 20;
constexpr size_t WS_SYNC = 20u << 20, WS_FQ = WS_SYNC + 4096, WS_FH = WS_SYNC + 8192, WS_FO = WS_SYNC + 12288, WS_FX = WS_SYNC + 16384;
constexpr size_t WS_ST = WS_SYNC + 32768;
constexpr unsigned IMG_BYTES = 2u << 20, H_BYTES = NTT * NFG * 1024;

constexpr int F_BASE = 131072, F_SLOT = 6400, F_NS = 5, F_CNT = F_BASE + 32512;
constexpr int CNT_KV = F_CNT, CNT_F = F_CNT + 64, CNT_UP = F_CNT + 96, CNT_P1 = F_CNT + 32, A_GO = F_CNT + 36, CNT_A0 = F_CNT + 40, A_GO2 = F_CNT + 44, TAG_W = F_CNT + 112;
#define MFMA(a, b, c) __builtin_amdgcn_mfma_f32_32x32x16_bf16(a, b, c, 0, 0, 0)
#define SRD(p, bytes) __builtin_amdgcn_make_buffer_rsrc((void*)(p), (short)0, (int)(bytes), 0x00020000)
typedef __amdgpu_buffer_rsrc_t srd_t;

__device__ __forceinline__ void dma16(unsigned lds_base, unsigned voff, srd_t srd, unsigned soff) {
  asm volatile("s_mov_b32 m0, %0\n\ts_nop 0\n\tbuffer_load_dwordx4 %1, %2, %3 offen lds" ::"s"(lds_base), "v"(voff), "s"(srd), "s"(soff) : "memory");
}
__device__ __forceinline__ void dma16_sc1(unsigned lds_base, unsigned voff, srd_t srd, unsigned soff) {
  asm volatile("s_mov_b32 m0, %0\n\ts_nop 0\n\tbuffer_load_dwordx4 %1, %2, %3 offen sc1 lds" ::"s"(lds_base), "v"(voff), "s"(srd), "s"(soff) : "memory");
}
template <int AUX>
__device__ __forceinline__ void store16(srd_t srd, unsigned voff, unsigned soff, u32x4 v) { __builtin_amdgcn_raw_buffer_store_b128(v, srd, voff + soff, 0, AUX); }
typedef __attribute__((address_space(1))) unsigned long long gu64;
typedef unsigned long long u64;
__device__ __forceinline__ void flag_store(char* p, u64 tag) { __hip_atomic_store((gu64*)p, tag, __ATOMIC_RELAXED, __HIP_MEMORY_SCOPE_AGENT); }
#define POLL_SLEEP 3
__device__ __forceinline__ void flags_wait(const char* p, bool mine, u64 tag) {
  for (unsigned spins = 0; spins < (1u << 16); ++spins) {
    const u64 v = __hip_atomic_load((gu64*)p, __ATOMIC_RELAXED, __HIP_MEMORY_SCOPE_AGENT);
    if (__all(!mine || v == tag)) break;
    __builtin_amdgcn_s_sleep(POLL_SLEEP);
  }
}
__device__ __forceinline__ s16x4 tr_read(lds_cptr p) {
  return __builtin_bit_cast(s16x4, __builtin_amdgcn_ds_read_tr16_b64_v4i16((LAS s16x4*)p));
}
__device__ __forceinline__ bf16x8 tr_frag(lds_cptr p, int second) {
  s16x4 lo = tr_read(p), hi = tr_read(p + second);
  return (bf16x8){lo[0], lo[1], lo[2], lo[3], hi[0], hi[1], hi[2], hi[3]};
}
__device__ __forceinline__ unsigned cvtpk(float lo, float hi) {
  bf16x2 r = {(__bf16)lo, (__bf16)hi};
  return __builtin_bit_cast(unsigned, r);
}
__device__ __forceinline__ float dot2(unsigned a, unsigned b, float c) {
  return __builtin_amdgcn_fdot2_f32_bf16(__builtin_bit_cast(bf16x2, a), __builtin_bit_cast(bf16x2, b), c, false);
}
__device__ __forceinline__ int fresh_lane() { int l; asm volatile("v_mbcnt_lo_u32_b32 %0, -1, 0\n\tv_mbcnt_hi_u32_b32 %0, -1, %0" : "=v"(l)); return l; }
__device__ __forceinline__ float bflo(unsigned v) { return __uint_as_float(v << 16); }
__device__ __forceinline__ float bfhi(unsigned v) { return __uint_as_float(v & 0xffff0000u); }
__device__ __forceinline__ float half_sum(float v) {
  auto r = __builtin_amdgcn_permlane32_swap(__float_as_uint(v), __float_as_uint(v), false, false);
  return __uint_as_float(r[0]) + __uint_as_float(r[1]);
}
__device__ __forceinline__ float half_max(float v) {
  auto r = __builtin_amdgcn_permlane32_swap(__float_as_uint(v), __float_as_uint(v), false, false);
  return __builtin_fmaxf(__uint_as_float(r[0]), __uint_as_float(r[1]));
}
__device__ __forceinline__ u32x4 pack8(const f32x16& v, int b) {
  return (u32x4){cvtpk(v[b], v[b + 1]), cvtpk(v[b + 2], v[b + 3]), cvtpk(v[b + 4], v[b + 5]), cvtpk(v[b + 6], v[b + 7])};
}
#define SB() __builtin_amdgcn_sched_barrier(0)
#define WAITV(n) asm volatile("s_waitcnt vmcnt(%0)" ::"i"(n) : "memory")
#define WAITV_BAR(n) asm volatile("s_waitcnt vmcnt(%0) lgkmcnt(0)\n\ts_barrier" ::"i"(n) : "memory")

#define STAMP(k, i)
#define TR(i)
constexpr int K1_SLOT = 28672, K1_NS = 5, K1_TAB = K1_NS * K1_SLOT, K1_LDS = K1_TAB + 2048;
#define K1ROT ((bi & 3) * 2)
#define PREFETCH 5
__device__ __forceinline__ void phase_qkv(char* lds, const u16* __restrict__ x, const u16* __restrict__ wqkv, char* __restrict__ ws, int wid, int lane, int xcd, int bi,
                                          const u16* w1, const u16* w3, const u16* w2, const u16* wo) {
  const int w = wid & 3;
  const int panel = 4 * (xcd & 3) + (bi & 3), g = 8 * (xcd >> 2) + (bi >> 2);
  const int tt = 4 * panel + w;
  const int h = lane >> 5, tok = lane & 31;
  const lds_cptr L = (lds_cptr)lds;
  const int grp1 = (lane >> 4) & 1, q = (lane & 15) >> 2, p = lane & 3;
  const lds_cptr wb = L + 16384 + (8 * h + q) * 64 + grp1 * 32 + p * 8;
  unsigned xb_[3][4], wb_[3];
#pragma unroll
  for (int sp = 0; sp < 3; ++sp) {
    wb_[sp] = (unsigned)(uintptr_t)wb + sp * 2 * K1_SLOT; asm volatile("" : "+v"(wb_[sp]));
#pragma unroll
    for (int ks = 0; ks < 4; ++ks) { xb_[sp][ks] = (unsigned)(uintptr_t)L + (32 * w + tok) * 128 + (((2 * ks + h) ^ ((tok >> 1) & 7)) * 16) + sp * 2 * K1_SLOT; asm volatile("" : "+v"(xb_[sp][ks])); }
  }
#define K1_XP(s, ks) ((lds_cptr)(uintptr_t)xb_[((s) % K1_NS) >> 1][ks] + (((s) % K1_NS) & 1) * K1_SLOT)
#define K1_WP(s) ((lds_cptr)(uintptr_t)wb_[((s) % K1_NS) >> 1] + (((s) % K1_NS) & 1) * K1_SLOT)
  LAS float* T = (LAS float*)lds;
  LAS float* cs = T;
  LAS float* st = T + 96 + w * 64;
  STAMP(0, 0);
  if (wid >= 8) {
    if (w == 0 && lane < 28) *(LAS unsigned*)(lds + F_CNT + lane * 4) = 0;
    const unsigned lds0 = (unsigned)(uintptr_t)lds;
    const srd_t sx = SRD(x, SEQ * DM * 2), sw = SRD(wqkv, DM * 3 * DM * 2);
    const unsigned vW = (lane >> 2) * 3072 + (lane & 3) * 16;
    const unsigned vXa = (lane >> 3) * 1024 + (((lane & 7) ^ (lane >> 4)) * 16);
    const unsigned vXb = (lane >> 3) * 1024 + (((lane & 7) ^ (4 + (lane >> 4))) * 16);
    auto issue = [&](int s) {
      const unsigned sl = lds0 + (s % K1_NS) * K1_SLOT;
      const int sk = (s + (K1ROT)) & 7;
#pragma unroll
      for (int i = 0; i < 3; ++i) {
        const int idx = 3 * w + i, tile = idx >> 2, part = idx & 3;
        dma16(sl + 16384 + tile * 4096 + part * 1024, vW, sw, (64 * sk + 16 * part) * 3072 + tile * 1024 + g * 64);
      }
#pragma unroll
      for (int i = 0; i < 4; ++i) dma16(sl + (32 * w + 8 * i) * 128, (i & 1) ? vXb : vXa, sx, (tt * 32 + 8 * i) * 1024 + sk * 128);
    };
    asm volatile("s_nop 4");
    issue(0); issue(1);
#pragma unroll
    for (int s = 0; s < 8; ++s) {
      if (s == 0) WAITV_BAR(7); else if (s <= 5) WAITV_BAR(14); else if (s == 6) WAITV_BAR(7); else WAITV_BAR(0);
      if (s == 0) issue(2);
      if (s + 3 < 8) issue(s + 3);
    }
    asm volatile("s_barrier" ::: "memory");
  } else if (wid >= 4) {
    const u32x4* xrow = (const u32x4*)(x + (size_t)(tt * 32 + tok) * DM + g * 32 + h * 8);
    const u32x4 xs0 = xrow[0], xs1 = xrow[2];
    u64 tag = 0;
    if (w == 0) tag = __hip_atomic_load((gu64*)(ws + WS_SYNC), __ATOMIC_RELAXED, __HIP_MEMORY_SCOPE_AGENT) + 1;
    f32x4 s1[2] = {}, s2[2] = {}, cc[2] = {};
    const bf16x8 ones = {0x3f80, 0x3f80, 0x3f80, 0x3f80, 0x3f80, 0x3f80, 0x3f80, 0x3f80};
    const int i16 = lane & 15, g4 = lane >> 4;
    unsigned xa_[3][4], wa_[3];
#pragma unroll
    for (int sp = 0; sp < 3; ++sp) {
#pragma unroll
      for (int u = 0; u < 4; ++u) { const int row = 32 * w + 16 * (u >> 1) + i16, c = 4 * (u & 1) + g4;
        xa_[sp][u] = (unsigned)(uintptr_t)L + row * 128 + ((c ^ ((row >> 1) & 7)) * 16) + sp * 2 * K1_SLOT; asm volatile("" : "+v"(xa_[sp][u])); }
      wa_[sp] = (unsigned)(uintptr_t)L + 16384 + (w < 3 ? w : 2) * 4096 + (8 * g4 + q) * 64 + p * 8 + sp * 2 * K1_SLOT; asm volatile("" : "+v"(wa_[sp]));
    }
    bf16x8 xa[2][4], wa[2][4];
#pragma unroll
    for (int s = 0; s < 9; ++s) {
      if (s < 8) {
        asm volatile("s_barrier" ::: "memory");
        if (s == PREFETCH) {
          const unsigned idx = (blockIdx.x * 4 + w) * 5;
#pragma unroll
          for (int i = 0; i < 5; ++i) {
            const unsigned kb = idx + i;
            const u16* base = kb < 1376 ? w1 : kb < 2752 ? w3 : kb < 4128 ? w2 : wo;
            const unsigned off = kb < 1376 ? kb : kb < 2752 ? kb - 1376 : kb < 4128 ? kb - 2752 : kb - 4128;
            dma16((unsigned)(uintptr_t)lds + 147456, lane * 16, SRD(base, kb < 4128 ? 1376 * 1024 : 512 * 1024), off * 1024);
          }
        }
        const int sp = (s % K1_NS) >> 1, so = ((s % K1_NS) & 1) * K1_SLOT;
#pragma unroll
        for (int u = 0; u < 4; ++u) {
          xa[s & 1][u] = *(LAS const bf16x8*)((lds_cptr)(uintptr_t)xa_[sp][u] + so);
          wa[s & 1][u] = tr_frag((lds_cptr)(uintptr_t)wa_[sp] + so + (u >> 1) * 32 + (u & 1) * 2048, 256);
        }
      }
      __builtin_amdgcn_sched_barrier(0);
      if (s > 0) {
        const int b = (s - 1) & 1;
#pragma unroll
        for (int u = 0; u < 4; ++u) {
          s2[u >> 1] = __builtin_amdgcn_mfma_f32_16x16x32_bf16(xa[b][u], xa[b][u], s2[u >> 1], 0, 0, 0);
          s1[u >> 1] = __builtin_amdgcn_mfma_f32_16x16x32_bf16(xa[b][u], ones, s1[u >> 1], 0, 0, 0);
          cc[u >> 1] = __builtin_amdgcn_mfma_f32_16x16x32_bf16(wa[b][u], ones, cc[u >> 1], 0, 0, 0);
        }
      }
      __builtin_amdgcn_sched_barrier(0);
    }
    {
      const int r = i16 & 3;
      const bool diag = (i16 >> 2) == g4;
#pragma unroll
      for (int th = 0; th < 2; ++th) {
        const float a1 = r == 0 ? s1[th][0] : r == 1 ? s1[th][1] : r == 2 ? s1[th][2] : s1[th][3];
        const float a2 = r == 0 ? s2[th][0] : r == 1 ? s2[th][1] : r == 2 ? s2[th][2] : s2[th][3];
        const float mean = a1 * (1.0f / DM), var = __builtin_fmaxf(a2 * (1.0f / DM) - mean * mean, 0.f);
        const float rs = __builtin_amdgcn_rsqf(var + 1e-5f);
        if (diag) { st[16 * th + i16] = rs; st[32 + 16 * th + i16] = -mean * rs; }
        if (w < 3 && i16 == 0) *(LAS f32x4*)(cs + w * 32 + 16 * th + 4 * g4) = cc[th];
      }
    }
    if (w == 0 && lane == 0) *(LAS u64*)(lds + TAG_W) = tag;
    asm volatile("s_waitcnt lgkmcnt(0)\n\ts_barrier" ::: "memory");
    const float rstd = st[tok], nm = st[32 + tok];
    const srd_t sXi = SRD(ws + WS_XN, IMG_BYTES);
    u32x4 y;
#pragma unroll
    for (int r = 0; r < 4; ++r) y[r] = cvtpk(bflo(xs0[r]) * rstd + nm, bfhi(xs0[r]) * rstd + nm);
    store16<16>(sXi, lane * 16, (tt * 32 + 2 * g) * 1024, y);
#pragma unroll
    for (int r = 0; r < 4; ++r) y[r] = cvtpk(bflo(xs1[r]) * rstd + nm, bfhi(xs1[r]) * rstd + nm);
    store16<16>(sXi, lane * 16, (tt * 32 + 2 * g + 1) * 1024, y);
  } else {
    f32x16 aq = {}, ak = {}, av = {};
    bf16x8 xf[3], wq[3], wk[3], wv[3];
#pragma unroll
    for (int n = 0; n < 34; ++n) {
      const int s = n >> 2, ks = n & 3, c = n % 3, m = (n + 1) % 3;
      if (n < 32 && ks == 0) { asm volatile("s_barrier" ::: "memory"); if (s == 0) STAMP(0, 1); }
      SB();
      if (n >= 2) aq = MFMA(wq[m], xf[m], aq);
      SB();
      if (n < 32) { xf[c] = *(LAS const bf16x8*)K1_XP(s, ks); wq[c] = tr_frag(K1_WP(s) + ks * 1024, 256); }
      SB();
      if (n >= 2) ak = MFMA(wk[m], xf[m], ak);
      SB();
      if (n < 32) wk[c] = tr_frag(K1_WP(s) + 4096 + ks * 1024, 256);
      SB();
      if (n >= 2) av = MFMA(xf[m], wv[m], av);
      SB();
      if (n < 32) wv[c] = tr_frag(K1_WP(s) + 8192 + ks * 1024, 256);
      SB();
    }
    STAMP(0, 2);
    asm volatile("s_barrier" ::: "memory");
    const float rstd = st[tok], nm = st[32 + tok], cv = cs[64 + tok];
    f32x4 cq4[4], ck4[4], r4[4], m4[4];
#pragma unroll
    for (int rg = 0; rg < 4; ++rg) { cq4[rg] = *(LAS const f32x4*)(cs + 8 * rg + 4 * h); ck4[rg] = *(LAS const f32x4*)(cs + 32 + 8 * rg + 4 * h);
      r4[rg] = *(LAS const f32x4*)(st + 8 * rg + 4 * h); m4[rg] = *(LAS const f32x4*)(st + 32 + 8 * rg + 4 * h); }
    const float QS = 0.25f * 1.4426950408889634f;
    const srd_t sQi = SRD(ws + WS_Q, IMG_BYTES), sKi = SRD(ws + WS_K, IMG_BYTES), sVi = SRD(ws + WS_V, IMG_BYTES);
    const unsigned so = ((2 * g) * 64 + tt) * 1024;
    f32x16 o;
    const float rq = rstd * QS, nq = nm * QS;
#pragma unroll
    for (int r = 0; r < 16; ++r) o[r] = ak[r] * rstd + nm * ck4[r >> 2][r & 3];
    store16<16>(sKi, lane * 16, so, pack8(o, 0));
    store16<16>(sKi, lane * 16, so + 65536, pack8(o, 8));
#pragma unroll
    for (int r = 0; r < 16; ++r) o[r] = av[r] * r4[r >> 2][r & 3] + m4[r >> 2][r & 3] * cv;
    const unsigned vv = grp1 * 65536 + h * 256 + (lane & 15) * 16;
    store16<16>(sVi, vv, so, pack8(o, 0));
    store16<16>(sVi, vv, so + 512, pack8(o, 8));
#pragma unroll
    for (int r = 0; r < 16; ++r) o[r] = aq[r] * rq + nq * cq4[r >> 2][r & 3];
    store16<16>(sQi, lane * 16, so, pack8(o, 0));
    store16<16>(sQi, lane * 16, so + 65536, pack8(o, 8));
  }
  if (wid < 8) {
    asm volatile("s_waitcnt vmcnt(0) lgkmcnt(0)" ::: "memory");
    unsigned old = 0;
    if (lane == 0) old = __hip_atomic_fetch_add((LAS unsigned*)(lds + CNT_P1), 1u, __ATOMIC_RELAXED, __HIP_MEMORY_SCOPE_WORKGROUP);
    if (__builtin_amdgcn_readfirstlane(old) == 7 && lane < 2)
      flag_store(ws + (lane ? WS_FX + (panel * 16 + g) * 8 : WS_FQ + (g * 16 + panel) * 8), *(LAS const u64*)(lds + TAG_W));
  }
  STAMP(0, 3);
}

constexpr float REF_OFF = 88.f;
#define ATT_UNROLL 8
#define STR2(x) #x
#define STR(x) STR2(x)
#define ATT_CLOB "v64","v65","v66","v67","v68","v69","v70","v71","v72","v73","v74","v75","v76","v77","v78","v79", \
  "v80","v81","v82","v83","v84","v85","v86","v87","v88","v89","v90","v91","v92","v93","v94","v95", \
  "v96","v97","v98","v99","v100","v101","v102","v103","v104","v105","v106","v107", \
  "v108","v109","v110","v111","v112","v113","v114","v115","v116","v117","v118","v119","v120","v121","v122","v123"
__device__ __forceinline__ void attn_fast(f32x16& o, const f32x16& c, const bf16x8& qf, unsigned kaddr, unsigned vaddr, unsigned cntaddr,
                                          srd_t sk, srd_t sv, unsigned voff, unsigned sL, unsigned sG, unsigned sQ) {
  const unsigned long long vmask = 0x0000ffff0000ffffull;
  const unsigned onev = (threadIdx.x & 15) == 0 ? 0x3f803f80u : 0u;
  asm volatile(
      ".macro ATT_DMA I\n"
      "s_add_u32 m0, %[sl], (((\\I)&1)*65536 + (2+((\\I)>>1))*8192)\n"
      "s_add_u32 s41, %[sq], ((2+((\\I)>>1))*8192)\n"
      "s_and_b32 s41, s41, 0xffff\n"
      "s_add_u32 s41, s41, %[sg]\n"
      ".if ((\\I)&1)\n"
      "buffer_load_dwordx4 %[vo], %[sv], s41 offen sc1 lds\n"
      ".else\n"
      "buffer_load_dwordx4 %[vo], %[sk], s41 offen sc1 lds\n"
      ".endif\n"
      ".endm\n"
      ".macro ATT_TILE X, Y, VC, VN, T, D\n"
      "s_waitcnt lgkmcnt(0)\n"
      "v_exp_f32 v[\\X+0], v[\\X+0]\n"
      "v_exp_f32 v[\\X+1], v[\\X+1]\n"
      "v_cvt_pk_bf16_f32 v102, v[\\Y+12], v[\\Y+13]\n"
      "v_exp_f32 v[\\X+2], v[\\X+2]\n"
      "v_cvt_pk_bf16_f32 v103, v[\\Y+14], v[\\Y+15]\n"
      "v_mfma_f32_32x32x16_bf16 v[\\Y:\\Y+15], v[104:107], %[q], %[c]\n"
      "ds_read_b128 v[104:107], %[ka] offset:(\\T+2)*1024\n"
      "v_exp_f32 v[\\X+3], v[\\X+3]\n"
      "v_cvt_pk_bf16_f32 v96, v[\\X+0], v[\\X+1]\n"
      "v_exp_f32 v[\\X+4], v[\\X+4]\n"
      "v_exp_f32 v[\\X+5], v[\\X+5]\n"
      "v_mfma_f32_32x32x16_bf16 %[o], v[\\VN+4:\\VN+7], v[100:103], %[o]\n"
      "s_mov_b64 exec, %[m]\n"
      "ds_read_b128 v[\\VN:\\VN+3], %[va] offset:(\\T+1)*1024\n"
      "ds_read_b128 v[\\VN+4:\\VN+7], %[va] offset:(\\T+1)*1024+512\n"
      "s_mov_b64 exec, -1\n"
      "v_cvt_pk_bf16_f32 v97, v[\\X+2], v[\\X+3]\n"
      "v_exp_f32 v[\\X+6], v[\\X+6]\n"
      ".if (\\D) >= 0\n ATT_DMA (\\D)\n .endif\n"
      "v_exp_f32 v[\\X+7], v[\\X+7]\n"
      "v_cvt_pk_bf16_f32 v98, v[\\X+4], v[\\X+5]\n"
      "v_exp_f32 v[\\X+8], v[\\X+8]\n"
      "v_exp_f32 v[\\X+9], v[\\X+9]\n"
      ".if (\\D) >= 0\n ATT_DMA (\\D)+1\n .endif\n"
      "v_cvt_pk_bf16_f32 v99, v[\\X+6], v[\\X+7]\n"
      "v_exp_f32 v[\\X+10], v[\\X+10]\n"
      "v_exp_f32 v[\\X+11], v[\\X+11]\n"
      "v_cvt_pk_bf16_f32 v100, v[\\X+8], v[\\X+9]\n"
      "v_mfma_f32_32x32x16_bf16 %[o], v[\\VC:\\VC+3], v[96:99], %[o]\n"
      "v_exp_f32 v[\\X+12], v[\\X+12]\n"
      "v_exp_f32 v[\\X+13], v[\\X+13]\n"
      ".if (\\D) >= 0\n ATT_DMA (\\D)+2\n .endif\n"
      "v_cvt_pk_bf16_f32 v101, v[\\X+10], v[\\X+11]\n"
      "v_exp_f32 v[\\X+14], v[\\X+14]\n"
      "v_exp_f32 v[\\X+15], v[\\X+15]\n"
      ".endm\n"
      ".set att_i, 108\n"
      ".rept 16\n"
      "v_mov_b32 v[att_i], %[one]\n"
      ".set att_i, att_i+1\n"
      ".endr\n"
      "v_mov_b32 v100, 0\n v_mov_b32 v101, 0\n v_mov_b32 v92, 0\n v_mov_b32 v93, 0\n v_mov_b32 v94, 0\n v_mov_b32 v95, 0\n"
      "ds_read_b128 v[104:107], %[ka]\n"
      "s_mov_b64 exec, %[m]\n"
      "ds_read_b128 v[108:111], %[va]\n"
      "ds_read_b128 v[112:115], %[va] offset:512\n"
      "s_mov_b64 exec, -1\n"
      "s_waitcnt lgkmcnt(2)\n"
      "v_mfma_f32_32x32x16_bf16 v[64:79], v[104:107], %[q], %[c]\n"
      "ds_read_b128 v[104:107], %[ka] offset:1024\n"
      "s_nop 7\n s_nop 7\n"
      "ATT_TILE 64, 80, 108, 116, 0, 0\n"
      "ATT_TILE 80, 64, 116, 108, 1, 3\n"
      "ATT_TILE 64, 80, 108, 116, 2, 6\n"
      "ATT_TILE 80, 64, 116, 108, 3, 9\n"
      "ATT_TILE 64, 80, 108, 116, 4, -1\n"
      "ATT_TILE 80, 64, 116, 108, 5, -1\n"
      "ATT_TILE 64, 80, 108, 116, 6, -1\n"
      "ATT_TILE 80, 64, 116, 108, 7, -1\n"
      "v_add_u32 %[ka], 8192, %[ka]\n"
      "v_add_u32 %[va], 8192, %[va]\n"
      "s_waitcnt vmcnt(0)\n"
      "v_mov_b32 v124, 1\n"
      "s_mov_b64 exec, 1\n"
      "ds_add_u32 %[cn], v124\n"
      "s_mov_b64 exec, -1\n"
      "s_mov_b32 s41, 0\n"
      "att_poll%=:\n"
      "ds_read_b32 v124, %[cn]\n"
      "s_add_u32 s41, s41, 1\n"
      "s_cmp_gt_u32 s41, 0x40000\n"
      "s_cbranch_scc1 att_go%=\n"
      "s_waitcnt lgkmcnt(0)\n"
      "v_readfirstlane_b32 s42, v124\n"
      "s_nop 0\n"
      "s_cmp_ge_u32 s42, 8\n"
      "s_cbranch_scc0 att_poll%=\n"
      "att_go%=:\n"
      "s_movk_i32 s40, 7\n"
      "att_loop%=:\n"
      ".set att_t, 0\n"
      ".rept 4\n"
      "ATT_TILE 64, 80, 108, 116, att_t, -1\n"
      "ATT_TILE 80, 64, 116, 108, att_t+1, -1\n"
      ".set att_t, att_t+2\n"
      ".endr\n"
      "v_add_u32 %[ka], 8192, %[ka]\n"
      "v_add_u32 %[va], 8192, %[va]\n"
      "s_sub_u32 s40, s40, 1\n"
      "s_cmp_lg_u32 s40, 0\n"
      "s_cbranch_scc1 att_loop%=\n"
      "s_waitcnt lgkmcnt(0)\n"
      "v_cvt_pk_bf16_f32 v102, v92, v93\n"
      "v_cvt_pk_bf16_f32 v103, v94, v95\n"
      "s_nop 1\n"
      "v_mfma_f32_32x32x16_bf16 %[o], v[120:123], v[100:103], %[o]\n"
      "s_nop 7\n s_nop 7\n"
      ".purgem ATT_TILE\n"
      ".purgem ATT_DMA\n"
      : [o] "+v"(o), [ka] "+v"(kaddr), [va] "+v"(vaddr)
      : [c] "v"(c), [q] "v"(qf), [m] "s"(vmask), [one] "v"(onev), [cn] "v"(cntaddr), [sk] "s"(sk), [sv] "s"(sv), [vo] "v"(voff), [sl] "s"(sL), [sg] "s"(sG), [sq] "s"(sQ)
      : "memory", "s40", "s41", "s42", "scc", "v124", ATT_CLOB);
}

__device__ __forceinline__ void lds_wait0(unsigned addr, unsigned want) {
  for (unsigned spins = 0; spins < (1u << 20); ++spins) {
    unsigned v; asm volatile("ds_read_b32 %0, %1\n\ts_waitcnt lgkmcnt(0)" : "=&v"(v) : "v"(addr) : "memory");
    if ((unsigned)__builtin_amdgcn_readfirstlane(v) >= want) break;
    __builtin_amdgcn_s_sleep(1);
  }
}
__device__ __forceinline__ void attn_safe(f32x16& o, bf16x8 qf, lds_cptr kp, lds_cptr vp, int lane) {
  o = f32x16{};
  float m = -INFINITY;
  const bf16x8 ones = {0x3f80, 0x3f80, 0x3f80, 0x3f80, 0x3f80, 0x3f80, 0x3f80, 0x3f80};
#pragma unroll 1
  for (int t = 0; t < NTT; ++t) {
    const bf16x8 kf = *(LAS const bf16x8*)(kp + t * 1024);
    f32x16 s = MFMA(kf, qf, f32x16{});
    float mx = s[0];
#pragma unroll
    for (int r = 1; r < 16; ++r) mx = __builtin_fmaxf(mx, s[r]);
    mx = __builtin_fmaxf(half_max(mx), m);
    const float a = __builtin_amdgcn_exp2f(m - mx);
    m = mx;
#pragma unroll
    for (int r = 0; r < 16; ++r) { o[r] *= a; s[r] = __builtin_amdgcn_exp2f(s[r] - m); }
    bf16x8 v0 = *(LAS const bf16x8*)(vp + t * 1024), v1 = *(LAS const bf16x8*)(vp + t * 1024 + 512);
    if (lane & 16) { v0 = ones; v1 = ones; }
    o = MFMA(v0, __builtin_bit_cast(bf16x8, pack8(s, 0)), o);
    o = MFMA(v1, __builtin_bit_cast(bf16x8, pack8(s, 8)), o);
  }
}

__device__ __forceinline__ u32x4 attn_wave(const char* lds, bf16x8 qf, int lane, unsigned cntaddr, srd_t sk, srd_t sv, unsigned sL, unsigned sG, unsigned sQ, unsigned go2) {
  const lds_cptr kp = (lds_cptr)lds + lane * 16;
  const lds_cptr vp = (lds_cptr)lds + 65536 + (lane >> 5) * 256 + (lane & 15) * 16;
  float mx;
  {
    const f32x16 s0 = MFMA(*(LAS const bf16x8*)(kp), qf, f32x16{}), s1 = MFMA(*(LAS const bf16x8*)(kp + 1024), qf, f32x16{});
    mx = __builtin_fmaxf(s0[0], s1[0]);
#pragma unroll
    for (int r = 1; r < 16; ++r) mx = __builtin_fmaxf(__builtin_fmaxf(mx, s0[r]), s1[r]);
    mx = half_max(mx);
  }
  const float nref = -(mx + REF_OFF);
  f32x16 c, o = {};
#pragma unroll
  for (int r = 0; r < 16; ++r) c[r] = nref;
  lds_wait0(go2, 1);
  attn_fast(o, c, qf, (unsigned)(uintptr_t)kp, (unsigned)(uintptr_t)vp, cntaddr, sk, sv, lane * 16, sL, sG, sQ);
  STAMP(1, 2);
  { auto r = __builtin_amdgcn_permlane32_swap(__float_as_uint(o[8]), __float_as_uint(o[8]), false, false); o[8] = __uint_as_float(r[0]); }
  float amax = 0.f; bool bad = !(o[8] < 0x1p126f);
#pragma unroll
  for (int r = 0; r < 8; ++r) { const float a = __builtin_fabsf(o[r]); bad |= !(a < 0x1p126f); amax = __builtin_fmaxf(amax, a); }
  bad |= !(amax > 0x1p-100f);
  if (__builtin_expect(__any(bad), 0)) attn_safe(o, qf, kp, vp, lane);
  asm volatile("s_waitcnt lgkmcnt(0)" ::: "memory");
  if (lane == 0) __hip_atomic_fetch_add((LAS unsigned*)(uintptr_t)(cntaddr + 12), 1u, __ATOMIC_RELAXED, __HIP_MEMORY_SCOPE_WORKGROUP);
  const float rl = __builtin_amdgcn_rcpf(o[8]);
#pragma unroll
  for (int r = 0; r < 8; ++r) o[r] *= rl;
  return pack8(o, 0);
}

__device__ __forceinline__ float nopk(float v) { asm("" : "+v"(v)); return v; }
__device__ __forceinline__ float silu(float v) { return v * __builtin_amdgcn_rcpf(1.0f + __builtin_amdgcn_exp2f(-1.4426950408889634f * v)); }
#define FPRIO 2
#define POLLLO 1
#define POLL_PRIO_LO "s_setprio 0\n"
#define POLL_PRIO_HI "s_setprio " STR(FPRIO) "\n"
__device__ __forceinline__ void fbar(unsigned cntaddr, unsigned target) {
  unsigned t, sv, sp;
  asm volatile(
      "v_mov_b32 %0, 1\n"
      "s_mov_b64 exec, 1\n"
      "ds_add_u32 %3, %0\n"
      "s_mov_b64 exec, -1\n"
      "s_mov_b32 %2, 0\n"
      POLL_PRIO_LO
      "fb_poll%=:\n"
      "ds_read_b32 %0, %3\n"
      "s_add_u32 %2, %2, 1\n"
      "s_cmp_gt_u32 %2, 0x40000\n"
      "s_cbranch_scc1 fb_done%=\n"
      "s_waitcnt lgkmcnt(0)\n"
      "v_readfirstlane_b32 %1, %0\n"
      "s_nop 0\n"
      "s_cmp_ge_u32 %1, %4\n"
      "s_cbranch_scc0 fb_poll%=\n"
      "fb_done%=:\n"
      POLL_PRIO_HI
      "s_waitcnt lgkmcnt(0)\n"
      : "=&v"(t), "=&s"(sv), "=&s"(sp) : "v"(cntaddr), "s"(target) : "memory", "scc");
}
__device__ __forceinline__ void f_arrive(unsigned cntaddr) {
  unsigned t;
  asm volatile("v_mov_b32 %0, 1\n\ts_mov_b64 exec, 1\n\tds_add_u32 %1, %0\n\ts_mov_b64 exec, -1" : "=&v"(t) : "v"(cntaddr) : "memory");
}
__device__ __forceinline__ void f_check(unsigned cntaddr, unsigned target) {
  unsigned t, sv, sp;
  asm volatile(
      "s_mov_b32 %2, 0\n"
      "fc_poll%=:\n"
      "ds_read_b32 %0, %3\n"
      "s_add_u32 %2, %2, 1\n"
      "s_cmp_gt_u32 %2, 0x40000\n"
      "s_cbranch_scc1 fc_done%=\n"
      "s_waitcnt lgkmcnt(0)\n"
      "v_readfirstlane_b32 %1, %0\n"
      "s_nop 0\n"
      "s_cmp_ge_u32 %1, %4\n"
      "s_cbranch_scc0 fc_poll%=\n"
      "fc_done%=:\n"
      "s_waitcnt lgkmcnt(0)\n"
      : "=&v"(t), "=&s"(sv), "=&s"(sp) : "v"(cntaddr), "s"(target) : "memory", "scc");
}
__device__ __forceinline__ void dma16_48(unsigned lds_base, unsigned voff, srd_t srd, unsigned soff) {
  const unsigned long long m48 = 0xffffffffffffull;
  asm volatile("s_mov_b32 m0, %0\n\ts_mov_b64 exec, %4\n\tbuffer_load_dwordx4 %1, %2, %3 offen lds\n\ts_mov_b64 exec, -1" ::"s"(lds_base), "v"(voff), "s"(srd), "s"(soff), "s"(m48) : "memory");
}
#define FSLEEP 1
#define DSLEEP 1
#define SLP(n) do { SB(); __builtin_amdgcn_s_sleep(n); SB(); } while (0)
#define FPACE() SLP(1)
#define FPACE_A() SLP(1)
#define DPACE() SLP(1)
#define DPACE_A() SLP(1)
__device__ __forceinline__ void ffn_up(char* lds, const u16* w1, const u16* w3, char* ws, int panel, int gi, int w, int lane, u64 tag, unsigned& nbar) {
  const unsigned lds0 = (unsigned)(uintptr_t)lds;
  const int tt0 = 8 * panel + 2 * w, h = lane >> 5;
  const int ntile = gi < 22 ? 3 : 2, t0 = gi < 22 ? 3 * gi : 66 + 2 * (gi - 22);
  const srd_t s1 = SRD(w1, DM * FF * 2), s3 = SRD(w3, DM * FF * 2), sx = SRD(ws + WS_XN, IMG_BYTES), sh = SRD(ws + WS_H, H_BYTES);
  const unsigned vw = (lane / 6) * (FF * 2) + (lane % 6) * 16, vx = lane * 16;
  auto issueW = [&](int j) {
    const unsigned so = (32 * j + 8 * w) * (FF * 2) + t0 * 32, dst = lds0 + F_BASE + (j % F_NS) * F_SLOT + w * 768;
    dma16_48(dst, vw, s1, so);
    dma16_48(dst + 3200, vw, s3, so);
  };
  u32x4 xr[2][4];
  auto issueX = [&](int j) {
    const unsigned so = (tt0 * 32 + 2 * j) * 1024;
    asm volatile("buffer_load_dwordx4 %0, %4, %5, %6 offen sc1\n\tbuffer_load_dwordx4 %1, %4, %5, %7 offen sc1\n\t"
                 "buffer_load_dwordx4 %2, %4, %5, %6 offen offset:1024 sc1\n\tbuffer_load_dwordx4 %3, %4, %5, %7 offen offset:1024 sc1"
                 : "=&v"(xr[j & 1][0]), "=&v"(xr[j & 1][1]), "=&v"(xr[j & 1][2]), "=&v"(xr[j & 1][3]) : "v"(vx), "s"(sx), "s"(so), "s"(so + 32768) : "memory");
  };
  const int grp1 = (lane >> 4) & 1, q = (lane & 15) >> 2, p = lane & 3;
  const lds_cptr wb = (lds_cptr)lds + F_BASE + (grp1 ? 3200 : 0) + (8 * h + q) * 96 + 8 * p;
  asm volatile("s_nop 4");
  issueW(0); issueW(1); issueW(2);
  if (w == 0) {
    lds_wait0(lds0 + CNT_P1, 8);
    flags_wait(ws + WS_FX + (2 * panel * 16 + (lane & 31)) * 8, lane < 32, tag);
  }
  nbar += 4; fbar(lds0 + CNT_F, nbar);
  STAMP(5, 0);
  issueX(0); issueX(1);
  f32x16 acc[2][3] = {};
  bf16x8 wf[2][3];
  const unsigned cu = lds0 + CNT_UP;
#define XWAIT(n, b) asm volatile("s_waitcnt vmcnt(%4) lgkmcnt(0)" : "+v"(xr[b][0]), "+v"(xr[b][1]), "+v"(xr[b][2]), "+v"(xr[b][3]) : "i"(n) : "memory")
  XWAIT(4, 0);
  f_arrive(cu); f_arrive(cu);
  f_check(cu, 8);
#pragma unroll
  for (int T = 0; T < 3; ++T) wf[0][T] = tr_frag(wb + T * 32, 384);
#pragma unroll
  for (int j = 0; j < 16; ++j) {
    if (j == 1) XWAIT(6, 1);
    else if (j >= 2) XWAIT((j + 2 <= 15 ? 2 : 0) + (j + 1 <= 15 ? 4 : 0), j & 1);
    if (j >= 1 && j + 1 <= 15) f_arrive(cu);
    SB();
#pragma unroll
    for (int T = 0; T < 3; ++T) {
      acc[0][T] = MFMA(wf[0][T], __builtin_bit_cast(bf16x8, xr[j & 1][0]), acc[0][T]);
      SB();
      wf[1][T] = tr_frag(wb + (j % F_NS) * F_SLOT + 1536 + T * 32, 384);
      FPACE_A();
      acc[1][T] = MFMA(wf[0][T], __builtin_bit_cast(bf16x8, xr[j & 1][1]), acc[1][T]);
      if (T < 2) FPACE();
    }
    SB();
    if (j < 15) {
      if (j >= 1) f_check(cu, 4 * (j + 2));
      if (j + 3 < 16) issueW(j + 3);
    }
    SB();
#pragma unroll
    for (int T = 0; T < 3; ++T) {
      acc[0][T] = MFMA(wf[1][T], __builtin_bit_cast(bf16x8, xr[j & 1][2]), acc[0][T]);
      FPACE_A();
      acc[1][T] = MFMA(wf[1][T], __builtin_bit_cast(bf16x8, xr[j & 1][3]), acc[1][T]);
      SB();
      if (j < 15) wf[0][T] = tr_frag(wb + ((j + 1) % F_NS) * F_SLOT + T * 32, 384);
      if (T < 2) FPACE();
    }
    SB();
    if (j + 2 < 16) issueX(j + 2);
    SB();
  }
  STAMP(5, 2);
  lane = fresh_lane();
#pragma unroll
  for (int T = 0; T < 3; ++T)
#pragma unroll
    for (int th = 0; th < 2; ++th) {
      f32x16 hv;
#pragma unroll
      for (int r = 0; r < 8; ++r) hv[r] = nopk(silu(acc[th][T][r]) * acc[th][T][8 + r]);
      if (T < ntile) store16<16>(sh, lane * 16, ((tt0 + th) * NFG + t0 + T) * 1024, pack8(hv, 0));
    }
}

__device__ __forceinline__ void lds_wait(unsigned addr, unsigned want) {
  for (unsigned spins = 0; spins < (1u << 18); ++spins) {
    unsigned v; asm volatile("ds_read_b32 %0, %1\n\ts_waitcnt lgkmcnt(0)" : "=&v"(v) : "v"(addr) : "memory");
    if ((unsigned)__builtin_amdgcn_readfirstlane(v) >= want) break;
    __builtin_amdgcn_s_sleep(4);
  }
}

constexpr int DN_NS = 3, DN_HD = 5;
__device__ __forceinline__ void ffn_down(char* lds, const u16* w2, char* ws, int tb, int nb, int w, int lane, f32x16& p00, f32x16& p01, f32x16& p10, f32x16& p11) {
  const unsigned lbase = (unsigned)(uintptr_t)lds, lds0 = lbase + F_BASE + w * (DN_NS * 2048);
  const int h = lane >> 5;
  const int k0 = w < 2 ? 22 * w : 44 + 21 * (w - 2), nst = w < 2 ? 22 : 21;
  const srd_t s2 = SRD(w2, FF * DM * 2), sh = SRD(ws + WS_H, H_BYTES);
  const unsigned vw = (lane >> 3) * 1024 + (((lane & 7) ^ (((lane >> 4) & 1) << 2)) * 16), vh = lane * 16;
  const int grp1 = (lane >> 4) & 1, q = (lane & 15) >> 2, p = lane & 3;
  const lds_cptr L = (lds_cptr)lds + F_BASE + w * (DN_NS * 2048);
  const lds_cptr b0 = L + (4 * h + q) * 128 + ((q >> 1) & 1) * 64 + grp1 * 32 + p * 8;
  const lds_cptr b1 = L + (4 * h + q) * 128 + (((q >> 1) & 1) ^ 1) * 64 + grp1 * 32 + p * 8;
  u32x4 hf[DN_HD][2];
  auto issueW = [&](int j) {
    const unsigned so = (k0 + (j < nst ? j : 0)) * 16 * 1024 + nb * 128;
    dma16(lds0 + (j % DN_NS) * 2048, vw, s2, so);
    dma16(lds0 + (j % DN_NS) * 2048 + 1024, vw, s2, so + 8192);
  };
  auto issueH = [&](int j) {
    const unsigned ho = j < nst ? (2 * tb * NFG + k0 + j) * 1024 : 0x7ff00000u;
    asm volatile("buffer_load_dwordx4 %0, %2, %3, %4 offen sc1\n\tbuffer_load_dwordx4 %1, %2, %3, %5 offen sc1"
                 : "=&v"(hf[j % DN_HD][0]), "=&v"(hf[j % DN_HD][1]) : "v"(vh), "s"(sh), "s"(ho), "s"(ho + (j < nst ? NFG * 1024 : 0)) : "memory");
  };
  asm volatile("s_nop 4");
  issueH(0); issueH(1); issueW(0); issueH(2); issueW(1); issueH(3); issueW(2);
  f32x16 a00 = {}, a01 = {}, a10 = {}, a11 = {};
  bf16x8 w0[2], w1f[2];
  asm volatile("s_waitcnt vmcnt(8)" : "+v"(hf[0][0]), "+v"(hf[0][1])::"memory");
  w0[0] = tr_frag(b0, 1024); w1f[0] = tr_frag(b1, 1024);
#pragma unroll
  for (int j = 0; j < 22; ++j) {
    const int c = j & 1, n = c ^ 1;
    if (j + 1 < 22) {
      const int young = j < 2 ? 4 : (j + 2 < 22 ? 4 : 0) + (j + 3 < 22 ? 2 : 0);
      asm volatile("s_waitcnt vmcnt(%2) lgkmcnt(0)" : "+v"(hf[(j + 1) % DN_HD][0]), "+v"(hf[(j + 1) % DN_HD][1]) : "i"(young) : "memory");
    } else asm volatile("s_waitcnt lgkmcnt(0)" ::: "memory");
    if (j + 3 < 22) issueW(j + 3);
    if (j + 4 < 22) issueH(j + 4);
    const bf16x8 h0 = __builtin_bit_cast(bf16x8, hf[j % DN_HD][0]), h1 = __builtin_bit_cast(bf16x8, hf[j % DN_HD][1]);
    SB();
    a00 = MFMA(w0[c], h0, a00);
    SB();
    if (j + 1 < 22) w0[n] = tr_frag(b0 + ((j + 1) % DN_NS) * 2048, 1024);
    DPACE_A();
    a01 = MFMA(w0[c], h1, a01);
    DPACE();
    a10 = MFMA(w1f[c], h0, a10);
    SB();
    if (j + 1 < 22) w1f[n] = tr_frag(b1 + ((j + 1) % DN_NS) * 2048, 1024);
    DPACE_A();
    a11 = MFMA(w1f[c], h1, a11);
    DPACE();
  }
  p00 = a00; p01 = a01; p10 = a10; p11 = a11;
}

__device__ __forceinline__ void final_tile(char* lds, const u16* x, const u16* wo, char* ws, u16* out, const f32x16& a00, const f32x16& a01, const f32x16& a10, const f32x16& a11,
                                           int panel, int tb, int nb, int w, int lane, unsigned& nbar, u64 tag) {
  const unsigned lds0 = (unsigned)(uintptr_t)lds, cnt = lds0 + CNT_F;
  const int nh = w >> 1, th = w & 1, tt = 2 * tb + th, h = lane >> 5;
  const srd_t so = SRD(wo, DM * DM * 2), sO = SRD(ws + WS_O, IMG_BYTES);
  const size_t ob = (size_t)(tt * 32 + (lane & 31)) * DM + nb * 64 + nh * 32 + 4 * h;
  u32x2 xv[4];
#pragma unroll
  for (int i = 0; i < 4; ++i) xv[i] = *(const u32x2*)(x + ob + 8 * i);
  lds_wait(lds0 + CNT_KV + 12, 8);
  const unsigned vw = (lane >> 3) * 1024 + (((lane & 7) ^ (((lane >> 4) & 1) << 2)) * 16);
#pragma unroll
  for (int i = 0; i < 16; ++i) dma16(lds0 + (4 * i + w) * 1024, vw, so, (4 * i + w) * 8192 + nb * 128);
  LAS f32x4* R = (LAS f32x4*)(lds + 65536);
#pragma unroll
  for (int qd = 0; qd < 4; ++qd) {
    const f32x16& snd = qd == 0 ? a00 : qd == 1 ? a01 : qd == 2 ? a10 : a11;
#pragma unroll
    for (int i = 0; i < 4; ++i) R[((qd * 4 + w) * 4 + i) * 64 + lane] = (f32x4){snd[4 * i], snd[4 * i + 1], snd[4 * i + 2], snd[4 * i + 3]};
  }
  asm volatile("s_waitcnt lgkmcnt(0)" ::: "memory");
  nbar += 4; fbar(cnt, nbar);
  f32x16 g;
#pragma unroll
  for (int i = 0; i < 4; ++i) {
    const f32x4 v0 = R[((w * 4 + 0) * 4 + i) * 64 + lane], v1 = R[((w * 4 + 1) * 4 + i) * 64 + lane], v2 = R[((w * 4 + 2) * 4 + i) * 64 + lane], v3 = R[((w * 4 + 3) * 4 + i) * 64 + lane];
#pragma unroll
    for (int e = 0; e < 4; ++e) g[4 * i + e] = silu((v0[e] + v1[e]) + (v2[e] + v3[e]));
  }
  const u32x4 g0 = pack8(g, 0), g1 = pack8(g, 8);
  if (w == 0) flags_wait(ws + WS_FO + (panel * 32 + (lane & 31)) * 8, lane < 32, tag);
  asm volatile("s_waitcnt vmcnt(0) lgkmcnt(0)" ::: "memory");
  nbar += 4; fbar(cnt, nbar);
  STAMP(2, 2);
  bf16x8 of[32];
#pragma unroll
  for (int hd = 0; hd < 32; ++hd) of[hd] = __builtin_bit_cast(bf16x8, __builtin_amdgcn_raw_buffer_load_b128(sO, lane * 16, (tt * 32 + hd) * 1024, 16));
  const int grp1 = (lane >> 4) & 1, q = (lane & 15) >> 2, p = lane & 3;
  const lds_cptr b = (lds_cptr)lds + (4 * h + q) * 128 + ((((q >> 1) & 1) ^ nh) * 64) + grp1 * 32 + p * 8;
  f32x16 a = {};
#pragma unroll
  for (int hd = 0; hd < 32; ++hd) a = MFMA(tr_frag(b + hd * 2048, 1024), of[hd], a);
#pragma unroll
  for (int i = 0; i < 4; ++i) {
    const unsigned ga = i < 2 ? g0[2 * i] : g1[2 * i - 4], gb = i < 2 ? g0[2 * i + 1] : g1[2 * i - 3];
    u32x2 y;
    y[0] = cvtpk(bflo(xv[i][0]) + a[4 * i] * bflo(ga), bfhi(xv[i][0]) + a[4 * i + 1] * bfhi(ga));
    y[1] = cvtpk(bflo(xv[i][1]) + a[4 * i + 2] * bflo(gb), bfhi(xv[i][1]) + a[4 * i + 3] * bfhi(gb));
    *(u32x2*)(out + ob + 8 * i) = y;
  }
}

constexpr int K_LDS = 163840;
__global__ __launch_bounds__(768) void recurrent_scan__mha_block__glu_gated_swiglu__prompt_kernel(
    const u16* __restrict__ x, const u16* __restrict__ wqkv, const u16* __restrict__ wo, const u16* __restrict__ w1, const u16* __restrict__ w3, const u16* __restrict__ w2,
    char* __restrict__ ws, u16* __restrict__ out) {
  extern __shared__ __attribute__((aligned(16))) char lds[];
  const int tid = threadIdx.x, lane = tid & 63;
  const int wid = __builtin_amdgcn_readfirstlane(tid >> 6);
  const int xcd = blockIdx.x & 7, bi = blockIdx.x >> 3;
  const unsigned lds0 = (unsigned)(uintptr_t)lds;
  phase_qkv(lds, x, wqkv, ws, wid, lane, xcd, bi, w1, w3, w2, wo);
  const u64 tag = *(LAS const u64*)(lds + TAG_W);
  if (wid < 8) {
    const int lane = fresh_lane();
    const int w = wid, hd = 4 * xcd + (bi >> 3), qb = bi & 7, tt = qb * 8 + w;
    const srd_t sk = SRD(ws + WS_K, IMG_BYTES), sv = SRD(ws + WS_V, IMG_BYTES), sq = SRD(ws + WS_Q, IMG_BYTES);
    STAMP(1, 0);
    const char* fq = ws + WS_FQ + (hd >> 1) * 128;
    if (w == 0) {
      lds_wait0(lds0 + CNT_P1, 8);
      flags_wait(fq + (2 * ((qb + ((lane >> 1) & 1)) & 7) + (lane & 1)) * 8, lane < 4, tag);
      if (lane == 0) *(volatile LAS unsigned*)(lds + A_GO) = 1;
    } else lds_wait0(lds0 + A_GO, 1);
    STAMP(4, 0);
    u32x4 qv;
    asm volatile("s_nop 4\n\tbuffer_load_dwordx4 %0, %1, %2, %3 offen sc1" : "=&v"(qv) : "v"(lane * 16), "s"(sq), "s"((hd * 64 + tt) * 1024) : "memory");
#pragma unroll
    for (int c = 0; c < 2; ++c) {
      const int cc = (c + qb) & 7;
      dma16_sc1(lds0 + (c * 8 + w) * 1024, lane * 16, sk, hd * 65536 + (cc * 8 + w) * 1024);
      dma16_sc1(lds0 + 65536 + (c * 8 + w) * 1024, lane * 16, sv, hd * 65536 + (cc * 8 + w) * 1024);
    }
    if (w == 0) {
      flags_wait(fq + (lane & 15) * 8, lane < 16, tag);
      if (lane == 0) *(volatile LAS unsigned*)(lds + A_GO2) = 1;
    }
    asm volatile("s_waitcnt vmcnt(0)" : "+v"(qv)::"memory");
    f_arrive(lds0 + CNT_A0); f_check(lds0 + CNT_A0, 8);
    STAMP(1, 1);
    const u32x4 ov = attn_wave(lds, __builtin_bit_cast(bf16x8, qv), lane, lds0 + CNT_KV, sk, sv, lds0 + w * 1024, hd * 65536 + w * 1024, qb * 8192, lds0 + A_GO2);
    store16<16>(SRD(ws + WS_O, IMG_BYTES), lane * 16, (tt * 32 + hd) * 1024, ov);
    asm volatile("s_waitcnt vmcnt(0) lgkmcnt(0)" ::: "memory");
    unsigned old = 0;
    if (lane == 0) old = __hip_atomic_fetch_add((LAS unsigned*)(lds + CNT_KV + 4), 1u, __ATOMIC_RELAXED, __HIP_MEMORY_SCOPE_WORKGROUP);
    if (__builtin_amdgcn_readfirstlane(old) == 7 && lane == 0) flag_store(ws + WS_FO + (qb * 32 + hd) * 8, tag);
    STAMP(1, 3);
  } else {
    const int w = wid - 8;
    if (FPRIO) __builtin_amdgcn_s_setprio(FPRIO);
    unsigned nbar = 0;
    STAMP(2, 0);
    ffn_up(lds, w1, w3, ws, xcd, bi, w, fresh_lane(), tag, nbar);
    const int lane = fresh_lane();
    asm volatile("s_waitcnt vmcnt(0) lgkmcnt(0)" ::: "memory");
    nbar += 4; fbar(lds0 + CNT_F, nbar);
    if (w == 0) {
      if (lane == 0) flag_store(ws + WS_FH + (xcd * 32 + bi) * 8, tag);
      flags_wait(ws + WS_FH + (xcd * 32 + (lane & 31)) * 8, lane < 32, tag);
    }
    nbar += 4; fbar(lds0 + CNT_F, nbar);
    STAMP(2, 1);
    const int tb = 4 * xcd + (bi >> 3), nb = bi & 7;
    f32x16 p00, p01, p10, p11;
    ffn_down(lds, w2, ws, tb, nb, w, fresh_lane(), p00, p01, p10, p11);
    STAMP(3, 0);
    final_tile(lds, x, wo, ws, out, p00, p01, p10, p11, xcd, tb, nb, w, fresh_lane(), nbar, tag);
    if (blockIdx.x == 0 && w == 0 && fresh_lane() == 0) flag_store(ws + WS_SYNC, tag);
    STAMP(2, 3);
  }
}

extern "C" void kernel_launch(void* const* d_in, const int* in_sizes, int n_in, void* d_out, int out_size, void* d_ws, size_t ws_size, hipStream_t stream) {
  const u16* x = (const u16*)d_in[0]; const u16* wqkv = (const u16*)d_in[1]; const u16* wo = (const u16*)d_in[2];
  const u16* w1 = (const u16*)d_in[4]; const u16* w3 = (const u16*)d_in[5]; const u16* w2 = (const u16*)d_in[6];
  (void)hipFuncSetAttribute((const void*)recurrent_scan__mha_block__glu_gated_swiglu__prompt_kernel, hipFuncAttributeMaxDynamicSharedMemorySize, K_LDS);
  recurrent_scan__mha_block__glu_gated_swiglu__prompt_kernel<<<256, 768, K_LDS, stream>>>(x, wqkv, wo, w1, w3, w2, (char*)d_ws, (u16*)d_out);
}
```

```cpp
#include <hip/hip_runtime.h>
#include <hip/hip_bf16.h>
#include <stdint.h>

typedef unsigned short u16;
typedef short s16x4 __attribute__((ext_vector_type(4)));
typedef short bf16x8 __attribute__((ext_vector_type(8)));
typedef float f32x16 __attribute__((ext_vector_type(16)));
typedef float f32x4 __attribute__((ext_vector_type(4)));
typedef unsigned u32x4 __attribute__((ext_vector_type(4)));
typedef unsigned u32x2 __attribute__((ext_vector_type(2)));
typedef __bf16 bf16x2 __attribute__((ext_vector_type(2)));
#define LAS __attribute__((address_space(3)))
typedef LAS const char* lds_cptr;

constexpr int SEQ = 2048, DM = 512, FF = 1376;
constexpr int NTT = 64;
constexpr int NFG = 86;
constexpr size_t WS_Q = 0, WS_K = 2u << 20, WS_V = 4u << 20, WS_XN = 6u << 20, WS_O = 8u << 20, WS_H = 12u << 20;
constexpr size_t WS_SYNC = 20u << 20, WS_FQ = WS_SYNC + 4096, WS_FH = WS_SYNC + 8192, WS_FO = WS_SYNC + 12288, WS_FX = WS_SYNC + 16384;
constexpr unsigned IMG_BYTES = 2u << 20, H_BYTES = NTT * NFG * 1024;


constexpr int F_BASE = 131072, F_SLOT = 6400, F_NS = 5, DEAD_LDS = F_BASE + F_NS * F_SLOT, F_CNT = F_BASE + 32512;
constexpr int CNT_KV = F_CNT,
              CNT_O = F_CNT + 4,
              CNT_KVD = F_CNT + 12,
              CNT_P1 = F_CNT + 32,
              A_GO = F_CNT + 36,
              CNT_A0 = F_CNT + 40,
              CNT_F = F_CNT + 64,
              CNT_UP = F_CNT + 96,
              TAG_W = F_CNT + 112;
#define MFMA(a, b, c) __builtin_amdgcn_mfma_f32_32x32x16_bf16(a, b, c, 0, 0, 0)
#define SRD(p, bytes) __builtin_amdgcn_make_buffer_rsrc((void*)(p), (short)0, (int)(bytes), 0x00020000)
typedef __amdgpu_buffer_rsrc_t srd_t;

__device__ __forceinline__ void dma16(unsigned lds_base, unsigned voff, srd_t srd, unsigned soff) {
  asm volatile("s_mov_b32 m0, %0\n\ts_nop 0\n\tbuffer_load_dwordx4 %1, %2, %3 offen lds" ::"s"(lds_base), "v"(voff), "s"(srd), "s"(soff) : "memory");
}
__device__ __forceinline__ void dma16_sc1(unsigned lds_base, unsigned voff, srd_t srd, unsigned soff) {
  asm volatile("s_mov_b32 m0, %0\n\ts_nop 0\n\tbuffer_load_dwordx4 %1, %2, %3 offen sc1 lds" ::"s"(lds_base), "v"(voff), "s"(srd), "s"(soff) : "memory");
}
__device__ __forceinline__ void dma4(unsigned lds_base, unsigned voff, srd_t srd, unsigned soff) {
  asm volatile("s_mov_b32 m0, %0\n\ts_nop 0\n\tbuffer_load_dword %1, %2, %3 offen lds" ::"s"(lds_base), "v"(voff), "s"(srd), "s"(soff) : "memory");
}
template <int AUX>
__device__ __forceinline__ void store16(srd_t srd, unsigned voff, unsigned soff, u32x4 v) { __builtin_amdgcn_raw_buffer_store_b128(v, srd, voff + soff, 0, AUX); }
typedef __attribute__((address_space(1))) unsigned long long gu64;
typedef unsigned long long u64;
__device__ __forceinline__ void flag_store(char* p, u64 tag) { __hip_atomic_store((gu64*)p, tag, __ATOMIC_RELAXED, __HIP_MEMORY_SCOPE_AGENT); }
__device__ __forceinline__ void flags_wait(const char* p, bool mine, u64 tag) {
  for (unsigned spins = 0; spins < (1u << 16); ++spins) {
    const u64 v = __hip_atomic_load((gu64*)p, __ATOMIC_RELAXED, __HIP_MEMORY_SCOPE_AGENT);
    if (__all(!mine || v == tag)) break;
    __builtin_amdgcn_s_sleep(1);
  }
}
__device__ __forceinline__ s16x4 tr_read(lds_cptr p) {
  return __builtin_bit_cast(s16x4, __builtin_amdgcn_ds_read_tr16_b64_v4i16((LAS s16x4*)p));
}
__device__ __forceinline__ bf16x8 tr_frag(lds_cptr p, int second) {
  s16x4 lo = tr_read(p), hi = tr_read(p + second);
  return (bf16x8){lo[0], lo[1], lo[2], lo[3], hi[0], hi[1], hi[2], hi[3]};
}
__device__ __forceinline__ unsigned cvtpk(float lo, float hi) {
  bf16x2 r = {(__bf16)lo, (__bf16)hi};
  return __builtin_bit_cast(unsigned, r);
}
__device__ __forceinline__ int fresh_lane() { int l; asm volatile("v_mbcnt_lo_u32_b32 %0, -1, 0\n\tv_mbcnt_hi_u32_b32 %0, -1, %0" : "=v"(l)); return l; }
__device__ __forceinline__ float bflo(unsigned v) { return __uint_as_float(v << 16); }
__device__ __forceinline__ float bfhi(unsigned v) { return __uint_as_float(v & 0xffff0000u); }
__device__ __forceinline__ float half_max(float v) {
  auto r = __builtin_amdgcn_permlane32_swap(__float_as_uint(v), __float_as_uint(v), false, false);
  return __builtin_fmaxf(__uint_as_float(r[0]), __uint_as_float(r[1]));
}
__device__ __forceinline__ u32x4 pack8(const f32x16& v, int b) {
  return (u32x4){cvtpk(v[b], v[b + 1]), cvtpk(v[b + 2], v[b + 3]), cvtpk(v[b + 4], v[b + 5]), cvtpk(v[b + 6], v[b + 7])};
}
#define SB() __builtin_amdgcn_sched_barrier(0)
#define WAITV_BAR(n) asm volatile("s_waitcnt vmcnt(%0) lgkmcnt(0)\n\ts_barrier" ::"i"(n) : "memory")

constexpr int K1_SLOT = 28672, K1_NS = 5;
__device__ __forceinline__ void phase_qkv(char* lds, const u16* __restrict__ x, const u16* __restrict__ wqkv, char* __restrict__ ws, int wid, int lane, int xcd, int bi,
                                          const u16* w1, const u16* w3, const u16* w2, const u16* wo) {
  const int w = wid & 3;
  const int panel = 4 * (xcd & 3) + (bi & 3), g = 8 * (xcd >> 2) + (bi >> 2);
  const int tt = 4 * panel + w;
  const int h = lane >> 5, tok = lane & 31;
  const lds_cptr L = (lds_cptr)lds;
  const int grp1 = (lane >> 4) & 1, q = (lane & 15) >> 2, p = lane & 3;
  const lds_cptr wb = L + 16384 + (8 * h + q) * 64 + grp1 * 32 + p * 8;
#define K1_XP(s, ks) ((lds_cptr)(uintptr_t)xb_[((s) % K1_NS) >> 1][ks] + (((s) % K1_NS) & 1) * K1_SLOT)
#define K1_WP(s) ((lds_cptr)(uintptr_t)wb_[((s) % K1_NS) >> 1] + (((s) % K1_NS) & 1) * K1_SLOT)
  LAS float* T = (LAS float*)lds;
  LAS float* cs = T;
  LAS float* st = T + 96 + w * 64;
  if (wid >= 8) {
    if (w == 0 && lane < 28) *(LAS unsigned*)(lds + F_CNT + lane * 4) = 0;
    const unsigned lds0 = (unsigned)(uintptr_t)lds;
    const srd_t sx = SRD(x, SEQ * DM * 2), sw = SRD(wqkv, DM * 3 * DM * 2);
    const unsigned vW = (lane >> 2) * 3072 + (lane & 3) * 16;
    const unsigned vXa = (lane >> 3) * 1024 + (((lane & 7) ^ (lane >> 4)) * 16);
    const unsigned vXb = (lane >> 3) * 1024 + (((lane & 7) ^ (4 + (lane >> 4))) * 16);
    auto issue = [&](int s) {
      const unsigned sl = lds0 + (s % K1_NS) * K1_SLOT;
      const int sk = (s + (bi & 3) * 2 + (xcd & 1)) & 7;
#pragma unroll
      for (int i = 0; i < 3; ++i) {
        const int idx = 3 * w + i, tile = idx >> 2, part = idx & 3;
        dma16(sl + 16384 + tile * 4096 + part * 1024, vW, sw, (64 * sk + 16 * part) * 3072 + tile * 1024 + g * 64);
      }
#pragma unroll
      for (int i = 0; i < 4; ++i) dma16(sl + (32 * w + 8 * i) * 128, (i & 1) ? vXb : vXa, sx, (tt * 32 + 8 * i) * 1024 + sk * 128);
    };
    asm volatile("s_nop 4");
    issue(0); issue(1);
#pragma unroll
    for (int s = 0; s < 8; ++s) {
      if (s <= 6) WAITV_BAR(7); else WAITV_BAR(0);
      if (s + 2 < 8) issue(s + 2);
    }
    asm volatile("s_barrier" ::: "memory");
  } else if (wid >= 4) {
    __builtin_amdgcn_s_setprio(1);
    const u32x4* xrow = (const u32x4*)(x + (size_t)(tt * 32 + tok) * DM + g * 32 + h * 8);
    const u32x4 xs0 = xrow[0], xs1 = xrow[2];
    u64 tag = 0;
    if (w == 0) tag = __hip_atomic_load((gu64*)(ws + WS_SYNC), __ATOMIC_RELAXED, __HIP_MEMORY_SCOPE_AGENT) + 1;
    f32x4 s1[2] = {}, s2[2] = {}, cc[2] = {};
    const bf16x8 ones = {0x3f80, 0x3f80, 0x3f80, 0x3f80, 0x3f80, 0x3f80, 0x3f80, 0x3f80};
    const int i16 = lane & 15, g4 = lane >> 4;
    unsigned xa_[3][4], wa_[3];
#pragma unroll
    for (int sp = 0; sp < 3; ++sp) {
#pragma unroll
      for (int u = 0; u < 4; ++u) { const int row = 32 * w + 16 * (u >> 1) + i16, c = 4 * (u & 1) + g4;
        xa_[sp][u] = (unsigned)(uintptr_t)L + row * 128 + ((c ^ ((row >> 1) & 7)) * 16) + sp * 2 * K1_SLOT; asm volatile("" : "+v"(xa_[sp][u])); }
      wa_[sp] = (unsigned)(uintptr_t)L + 16384 + (w < 3 ? w : 2) * 4096 + (8 * g4 + q) * 64 + p * 8 + sp * 2 * K1_SLOT; asm volatile("" : "+v"(wa_[sp]));
    }
    bf16x8 xa[2][4], wa[2][4];
#pragma unroll
    for (int s = 0; s < 8; ++s) {
      {
        asm volatile("s_barrier" ::: "memory");
        if (s == 5) {
#pragma unroll
          for (int i = 0; i < 2; ++i) {
            const unsigned pc = (blockIdx.x * 4 + w) + 1024 * i;
            const u16* base = pc < 344 ? w1 : pc < 688 ? w3 : pc < 1032 ? w2 : wo;
            const unsigned off = pc < 344 ? pc : pc < 688 ? pc - 344 : pc < 1032 ? pc - 688 : pc - 1032;
            if (pc < 1160) dma4((unsigned)(uintptr_t)lds + DEAD_LDS, lane * 64, SRD(base, pc < 1032 ? FF * DM * 2 : DM * DM * 2), off * 4096);
          }
        }
        const int sp = (s % K1_NS) >> 1, so = ((s % K1_NS) & 1) * K1_SLOT;
#pragma unroll
        for (int u = 0; u < 4; ++u) {
          xa[s & 1][u] = *(LAS const bf16x8*)((lds_cptr)(uintptr_t)xa_[sp][u] + so);
          wa[s & 1][u] = tr_frag((lds_cptr)(uintptr_t)wa_[sp] + so + (u >> 1) * 32 + (u & 1) * 2048, 256);
        }
      }
      SB();
      auto stat_mfma = [&](int b) {
#pragma unroll
        for (int u = 0; u < 4; ++u) {
          s2[u >> 1] = __builtin_amdgcn_mfma_f32_16x16x32_bf16(xa[b][u], xa[b][u], s2[u >> 1], 0, 0, 0);
          s1[u >> 1] = __builtin_amdgcn_mfma_f32_16x16x32_bf16(xa[b][u], ones, s1[u >> 1], 0, 0, 0);
          cc[u >> 1] = __builtin_amdgcn_mfma_f32_16x16x32_bf16(wa[b][u], ones, cc[u >> 1], 0, 0, 0);
        }
      };
      if (s > 0 && s < 7) stat_mfma((s - 1) & 1);
      if (s >= 6) stat_mfma(s & 1);
      SB();
    }
    {
      const int r = i16 & 3;
      const bool diag = (i16 >> 2) == g4;
#pragma unroll
      for (int th = 0; th < 2; ++th) {
        const float a1 = r == 0 ? s1[th][0] : r == 1 ? s1[th][1] : r == 2 ? s1[th][2] : s1[th][3];
        const float a2 = r == 0 ? s2[th][0] : r == 1 ? s2[th][1] : r == 2 ? s2[th][2] : s2[th][3];
        const float mean = a1 * (1.0f / DM), var = __builtin_fmaxf(a2 * (1.0f / DM) - mean * mean, 0.f);
        const float rs = __builtin_amdgcn_rsqf(var + 1e-5f);
        if (diag) { st[16 * th + i16] = rs; st[32 + 16 * th + i16] = -mean * rs; }
        if (w < 3 && i16 == 0) *(LAS f32x4*)(cs + w * 32 + 16 * th + 4 * g4) = cc[th];
      }
    }
    if (w == 0 && lane == 0) *(LAS u64*)(lds + TAG_W) = tag;
    asm volatile("s_waitcnt lgkmcnt(0)\n\ts_barrier" ::: "memory");
    const float rstd = st[tok], nm = st[32 + tok];
    const srd_t sXi = SRD(ws + WS_XN, IMG_BYTES);
    u32x4 y;
#pragma unroll
    for (int r = 0; r < 4; ++r) y[r] = cvtpk(bflo(xs0[r]) * rstd + nm, bfhi(xs0[r]) * rstd + nm);
    store16<16>(sXi, lane * 16, (tt * 32 + 2 * g) * 1024, y);
#pragma unroll
    for (int r = 0; r < 4; ++r) y[r] = cvtpk(bflo(xs1[r]) * rstd + nm, bfhi(xs1[r]) * rstd + nm);
    store16<16>(sXi, lane * 16, (tt * 32 + 2 * g + 1) * 1024, y);
  } else {
    unsigned xb_[3][4], wb_[3];
#pragma unroll
    for (int sp = 0; sp < 3; ++sp) {
      wb_[sp] = (unsigned)(uintptr_t)wb + sp * 2 * K1_SLOT; asm volatile("" : "+v"(wb_[sp]));
#pragma unroll
      for (int ks = 0; ks < 4; ++ks) { xb_[sp][ks] = (unsigned)(uintptr_t)L + (32 * w + tok) * 128 + (((2 * ks + h) ^ ((tok >> 1) & 7)) * 16) + sp * 2 * K1_SLOT; asm volatile("" : "+v"(xb_[sp][ks])); }
    }
    f32x16 aq = {}, ak = {}, av = {};
    bf16x8 xf[3], wq[3], wk[3], wv[3];
#pragma unroll
    for (int n = 0; n < 34; ++n) {
      const int s = n >> 2, ks = n & 3, c = n % 3, m = (n + 1) % 3;
      if (n < 32 && ks == 0) { asm volatile("s_barrier" ::: "memory"); }
      SB();
      if (n >= 2) aq = MFMA(wq[m], xf[m], aq);
      SB();
      if (n < 32) { xf[c] = *(LAS const bf16x8*)K1_XP(s, ks); wq[c] = tr_frag(K1_WP(s) + ks * 1024, 256); }
      SB();
      if (n >= 2) ak = MFMA(wk[m], xf[m], ak);
      SB();
      if (n < 32) wk[c] = tr_frag(K1_WP(s) + 4096 + ks * 1024, 256);
      SB();
      if (n >= 2) av = MFMA(xf[m], wv[m], av);
      SB();
      if (n < 32) wv[c] = tr_frag(K1_WP(s) + 8192 + ks * 1024, 256);
      SB();
    }
    asm volatile("s_barrier" ::: "memory");
    __builtin_amdgcn_s_setprio(2);
    const float rstd = st[tok], nm = st[32 + tok], cv = cs[64 + tok];
    f32x4 cq4[4], ck4[4], r4[4], m4[4];
#pragma unroll
    for (int rg = 0; rg < 4; ++rg) { cq4[rg] = *(LAS const f32x4*)(cs + 8 * rg + 4 * h); ck4[rg] = *(LAS const f32x4*)(cs + 32 + 8 * rg + 4 * h);
      r4[rg] = *(LAS const f32x4*)(st + 8 * rg + 4 * h); m4[rg] = *(LAS const f32x4*)(st + 32 + 8 * rg + 4 * h); }
    const float QS = 0.25f * 1.4426950408889634f;
    const srd_t sQi = SRD(ws + WS_Q, IMG_BYTES), sKi = SRD(ws + WS_K, IMG_BYTES), sVi = SRD(ws + WS_V, IMG_BYTES);
    const unsigned so = ((2 * g) * 64 + tt) * 1024;
    f32x16 o;
    const float rq = rstd * QS, nq = nm * QS;
#pragma unroll
    for (int r = 0; r < 16; ++r) o[r] = ak[r] * rstd + nm * ck4[r >> 2][r & 3];
    store16<16>(sKi, lane * 16, so, pack8(o, 0));
    store16<16>(sKi, lane * 16, so + 65536, pack8(o, 8));
#pragma unroll
    for (int r = 0; r < 16; ++r) o[r] = av[r] * r4[r >> 2][r & 3] + m4[r >> 2][r & 3] * cv;
    const unsigned vv = grp1 * 65536 + h * 256 + (lane & 15) * 16;
    store16<16>(sVi, vv, so, pack8(o, 0));
    store16<16>(sVi, vv, so + 512, pack8(o, 8));
#pragma unroll
    for (int r = 0; r < 16; ++r) o[r] = aq[r] * rq + nq * cq4[r >> 2][r & 3];
    store16<16>(sQi, lane * 16, so, pack8(o, 0));
    store16<16>(sQi, lane * 16, so + 65536, pack8(o, 8));
  }
  if (wid < 8) {
    asm volatile("s_waitcnt vmcnt(0) lgkmcnt(0)" ::: "memory");
    unsigned old = 0;
    if (lane == 0) old = __hip_atomic_fetch_add((LAS unsigned*)(lds + CNT_P1), 1u, __ATOMIC_RELAXED, __HIP_MEMORY_SCOPE_WORKGROUP);
    if (__builtin_amdgcn_readfirstlane(old) == 7 && lane < 2)
      flag_store(ws + (lane ? WS_FX + (panel * 16 + g) * 8 : WS_FQ + (g * 16 + panel) * 8), *(LAS const u64*)(lds + TAG_W));
  }
}

constexpr float REF_OFF = 88.f;
#define ATT_CLOB "v64","v65","v66","v67","v68","v69","v70","v71","v72","v73","v74","v75","v76","v77","v78","v79", \
  "v80","v81","v82","v83","v84","v85","v86","v87","v88","v89","v90","v91","v92","v93","v94","v95", \
  "v96","v97","v98","v99","v100","v101","v102","v103","v104","v105","v106","v107", \
  "v108","v109","v110","v111","v112","v113","v114","v115","v116","v117","v118","v119","v120","v121","v122","v123"
__device__ __forceinline__ void attn_fast(f32x16& o, const bf16x8& qf, unsigned kaddr, unsigned vaddr, unsigned cntaddr,
                                          srd_t sk, srd_t sv, unsigned voff, unsigned sL, unsigned sG, unsigned sQ) {
  const unsigned long long vmask = 0x0000ffff0000ffffull;
  const unsigned onev = (threadIdx.x & 15) == 0 ? 0x3f803f80u : 0u;
  asm volatile(
      ".macro ATT_DMA I\n"
      "s_add_u32 m0, %[sl], (((\\I)&1)*65536 + (2+((\\I)>>1))*8192)\n"
      "s_add_u32 s41, %[sq], ((2+((\\I)>>1))*8192)\n"
      "s_and_b32 s41, s41, 0xffff\n"
      "s_add_u32 s41, s41, %[sg]\n"
      ".if ((\\I)&1)\n"
      "buffer_load_dwordx4 %[vo], %[sv], s41 offen sc1 lds\n"
      ".else\n"
      "buffer_load_dwordx4 %[vo], %[sk], s41 offen sc1 lds\n"
      ".endif\n"
      ".endm\n"
      ".macro ATT_TILE X, Y, VC, VN, T, D\n"
      "s_waitcnt lgkmcnt(0)\n"
      "v_exp_f32 v[\\X+0], v[\\X+0]\n"
      "v_exp_f32 v[\\X+1], v[\\X+1]\n"
      "v_cvt_pk_bf16_f32 v102, v[\\Y+12], v[\\Y+13]\n"
      "v_exp_f32 v[\\X+2], v[\\X+2]\n"
      "v_cvt_pk_bf16_f32 v103, v[\\Y+14], v[\\Y+15]\n"
      "v_mfma_f32_32x32x16_bf16 v[\\Y:\\Y+15], v[104:107], %[q], v[128:143]\n"
      "ds_read_b128 v[104:107], %[ka] offset:(\\T+2)*1024\n"
      "v_exp_f32 v[\\X+3], v[\\X+3]\n"
      "v_cvt_pk_bf16_f32 v96, v[\\X+0], v[\\X+1]\n"
      "v_exp_f32 v[\\X+4], v[\\X+4]\n"
      "v_exp_f32 v[\\X+5], v[\\X+5]\n"
      "v_mfma_f32_32x32x16_bf16 %[o], v[\\VN+4:\\VN+7], v[100:103], %[o]\n"
      "s_mov_b64 exec, %[m]\n"
      "ds_read_b128 v[\\VN:\\VN+3], %[va] offset:(\\T+1)*1024\n"
      "ds_read_b128 v[\\VN+4:\\VN+7], %[va] offset:(\\T+1)*1024+512\n"
      "s_mov_b64 exec, -1\n"
      "v_cvt_pk_bf16_f32 v97, v[\\X+2], v[\\X+3]\n"
      "v_exp_f32 v[\\X+6], v[\\X+6]\n"
      ".if (\\D) >= 0\n ATT_DMA (\\D)\n .endif\n"
      "v_exp_f32 v[\\X+7], v[\\X+7]\n"
      "v_cvt_pk_bf16_f32 v98, v[\\X+4], v[\\X+5]\n"
      "v_exp_f32 v[\\X+8], v[\\X+8]\n"
      "v_exp_f32 v[\\X+9], v[\\X+9]\n"
      ".if (\\D) >= 0\n ATT_DMA (\\D)+1\n .endif\n"
      "v_cvt_pk_bf16_f32 v99, v[\\X+6], v[\\X+7]\n"
      "v_exp_f32 v[\\X+10], v[\\X+10]\n"
      "v_exp_f32 v[\\X+11], v[\\X+11]\n"
      "v_cvt_pk_bf16_f32 v100, v[\\X+8], v[\\X+9]\n"
      "v_mfma_f32_32x32x16_bf16 %[o], v[\\VC:\\VC+3], v[96:99], %[o]\n"
      "v_exp_f32 v[\\X+12], v[\\X+12]\n"
      "v_exp_f32 v[\\X+13], v[\\X+13]\n"
      ".if (\\D) >= 0\n ATT_DMA (\\D)+2\n .endif\n"
      "v_cvt_pk_bf16_f32 v101, v[\\X+10], v[\\X+11]\n"
      "v_exp_f32 v[\\X+14], v[\\X+14]\n"
      "v_exp_f32 v[\\X+15], v[\\X+15]\n"
      ".endm\n"
      "ds_read_b128 v[104:107], %[ka]\n"
      "ds_read_b128 v[96:99], %[ka] offset:1024\n"
      ".set att_i, 108\n"
      ".rept 16\n"
      "v_mov_b32 v[att_i], %[one]\n"
      ".set att_i, att_i+1\n"
      ".endr\n"
      "s_mov_b64 exec, %[m]\n"
      "ds_read_b128 v[108:111], %[va]\n"
      "ds_read_b128 v[112:115], %[va] offset:512\n"
      "s_mov_b64 exec, -1\n"
      "s_waitcnt lgkmcnt(2)\n"
      "v_mfma_f32_32x32x16_bf16 v[64:79], v[104:107], %[q], 0\n"
      "v_mfma_f32_32x32x16_bf16 v[80:95], v[96:99], %[q], 0\n"
      "v_mov_b32 v104, v96\n v_mov_b32 v105, v97\n v_mov_b32 v106, v98\n v_mov_b32 v107, v99\n"
      "v_mov_b32 v100, 0\n v_mov_b32 v101, 0\n"
      "s_nop 7\n"
      "v_max3_f32 v124, v64, v65, v66\n"
      "v_max3_f32 v125, v67, v68, v69\n"
      "v_max3_f32 v124, v124, v70, v71\n"
      "v_max3_f32 v125, v125, v72, v73\n"
      "v_max3_f32 v124, v124, v74, v75\n"
      "v_max3_f32 v125, v125, v76, v77\n"
      "v_max3_f32 v124, v124, v78, v79\n"
      "v_max3_f32 v125, v125, v80, v81\n"
      "v_max3_f32 v124, v124, v82, v83\n"
      "v_max3_f32 v125, v125, v84, v85\n"
      "v_max3_f32 v124, v124, v86, v87\n"
      "v_max3_f32 v125, v125, v88, v89\n"
      "v_max3_f32 v124, v124, v90, v91\n"
      "v_max3_f32 v125, v125, v92, v93\n"
      "v_max3_f32 v124, v124, v94, v95\n"
      "v_max_f32 v124, v124, v125\n"
      "v_mov_b32 v92, 0\n v_mov_b32 v93, 0\n"
      "v_mov_b32 v125, v124\n"
      "v_mov_b32 v94, 0\n v_mov_b32 v95, 0\n"
      "v_permlane32_swap_b32 v124, v125\n"
      "s_nop 1\n"
      "v_max_f32 v124, v124, v125\n"
      "v_sub_f32 v124, %[off], v124\n"
      ".set att_i, 0\n"
      ".rept 16\n"
      "v_mov_b32 v[128+att_i], v124\n"
      "v_add_f32 v[64+att_i], v124, v[64+att_i]\n"
      ".set att_i, att_i+1\n"
      ".endr\n"
      "ATT_TILE 64, 80, 108, 116, 0, 0\n"
      "ATT_TILE 80, 64, 116, 108, 1, 3\n"
      "ATT_TILE 64, 80, 108, 116, 2, 6\n"
      "ATT_TILE 80, 64, 116, 108, 3, 9\n"
      "ATT_TILE 64, 80, 108, 116, 4, -1\n"
      "ATT_TILE 80, 64, 116, 108, 5, -1\n"
      "ATT_TILE 64, 80, 108, 116, 6, -1\n"
      "ATT_TILE 80, 64, 116, 108, 7, -1\n"
      "v_add_u32 %[ka], 8192, %[ka]\n"
      "v_add_u32 %[va], 8192, %[va]\n"
      "s_waitcnt vmcnt(0)\n"
      "v_mov_b32 v124, 1\n"
      "s_mov_b64 exec, 1\n"
      "ds_add_u32 %[cn], v124\n"
      "s_mov_b64 exec, -1\n"
      "s_mov_b32 s41, 0\n"
      "att_poll%=:\n"
      "ds_read_b32 v124, %[cn]\n"
      "s_add_u32 s41, s41, 1\n"
      "s_cmp_gt_u32 s41, 0x40000\n"
      "s_cbranch_scc1 att_go%=\n"
      "s_waitcnt lgkmcnt(0)\n"
      "v_readfirstlane_b32 s42, v124\n"
      "s_nop 0\n"
      "s_cmp_ge_u32 s42, 8\n"
      "s_cbranch_scc0 att_poll%=\n"
      "att_go%=:\n"
      ".rept 7\n"
      ".set att_t, 0\n"
      ".rept 4\n"
      "ATT_TILE 64, 80, 108, 116, att_t, -1\n"
      "ATT_TILE 80, 64, 116, 108, att_t+1, -1\n"
      ".set att_t, att_t+2\n"
      ".endr\n"
      "v_add_u32 %[ka], 8192, %[ka]\n"
      "v_add_u32 %[va], 8192, %[va]\n"
      ".endr\n"
      "s_waitcnt lgkmcnt(0)\n"
      "v_cvt_pk_bf16_f32 v102, v92, v93\n"
      "v_cvt_pk_bf16_f32 v103, v94, v95\n"
      "s_nop 1\n"
      "v_mfma_f32_32x32x16_bf16 %[o], v[120:123], v[100:103], %[o]\n"
      "s_nop 7\n s_nop 7\n"
      ".purgem ATT_TILE\n"
      ".purgem ATT_DMA\n"
      : [o] "+v"(o), [ka] "+v"(kaddr), [va] "+v"(vaddr)
      : [off] "s"(-REF_OFF), [q] "v"(qf), [m] "s"(vmask), [one] "v"(onev), [cn] "v"(cntaddr), [sk] "s"(sk), [sv] "s"(sv), [vo] "v"(voff), [sl] "s"(sL), [sg] "s"(sG), [sq] "s"(sQ)
      : "memory", "s40", "s41", "s42", "scc", "v124", "v125", ATT_CLOB,
        "v128", "v129", "v130", "v131", "v132", "v133", "v134", "v135", "v136", "v137", "v138", "v139", "v140", "v141", "v142", "v143");
}

__device__ __forceinline__ void lds_wait0(unsigned addr, unsigned want) {
  for (unsigned spins = 0; spins < (1u << 20); ++spins) {
    unsigned v; asm volatile("ds_read_b32 %0, %1\n\ts_waitcnt lgkmcnt(0)" : "=&v"(v) : "v"(addr) : "memory");
    if ((unsigned)__builtin_amdgcn_readfirstlane(v) >= want) break;
    __builtin_amdgcn_s_sleep(1);
  }
}
__device__ __forceinline__ void attn_safe(f32x16& o, bf16x8 qf, lds_cptr kp, lds_cptr vp, int lane) {
  o = f32x16{};
  float m = -INFINITY;
  const bf16x8 ones = {0x3f80, 0x3f80, 0x3f80, 0x3f80, 0x3f80, 0x3f80, 0x3f80, 0x3f80};
#pragma unroll 1
  for (int t = 0; t < NTT; ++t) {
    const bf16x8 kf = *(LAS const bf16x8*)(kp + t * 1024);
    f32x16 s = MFMA(kf, qf, f32x16{});
    float mx = s[0];
#pragma unroll
    for (int r = 1; r < 16; ++r) mx = __builtin_fmaxf(mx, s[r]);
    mx = __builtin_fmaxf(half_max(mx), m);
    const float a = __builtin_amdgcn_exp2f(m - mx);
    m = mx;
#pragma unroll
    for (int r = 0; r < 16; ++r) { o[r] *= a; s[r] = __builtin_amdgcn_exp2f(s[r] - m); }
    bf16x8 v0 = *(LAS const bf16x8*)(vp + t * 1024), v1 = *(LAS const bf16x8*)(vp + t * 1024 + 512);
    if (lane & 16) { v0 = ones; v1 = ones; }
    o = MFMA(v0, __builtin_bit_cast(bf16x8, pack8(s, 0)), o);
    o = MFMA(v1, __builtin_bit_cast(bf16x8, pack8(s, 8)), o);
  }
}

__device__ __forceinline__ u32x4 attn_wave(const char* lds, bf16x8 qf, int lane, unsigned cntaddr, srd_t sk, srd_t sv, unsigned sL, unsigned sG, unsigned sQ) {
  const lds_cptr kp = (lds_cptr)lds + lane * 16;
  const lds_cptr vp = (lds_cptr)lds + 65536 + (lane >> 5) * 256 + (lane & 15) * 16;
  f32x16 o = {};
  attn_fast(o, qf, (unsigned)(uintptr_t)kp, (unsigned)(uintptr_t)vp, cntaddr, sk, sv, lane * 16, sL, sG, sQ);
  { auto r = __builtin_amdgcn_permlane32_swap(__float_as_uint(o[8]), __float_as_uint(o[8]), false, false); o[8] = __uint_as_float(r[0]); }
  float amax = 0.f; bool bad = !(o[8] < 0x1p126f);
#pragma unroll
  for (int r = 0; r < 8; ++r) { const float a = __builtin_fabsf(o[r]); bad |= !(a < 0x1p126f); amax = __builtin_fmaxf(amax, a); }
  bad |= !(amax > 0x1p-100f);
  if (__builtin_expect(__any(bad), 0)) attn_safe(o, qf, kp, vp, lane);
  const float rl = __builtin_amdgcn_rcpf(o[8]);
#pragma unroll
  for (int r = 0; r < 8; ++r) o[r] *= rl;
  return pack8(o, 0);
}

__device__ __forceinline__ float nopk(float v) { asm("" : "+v"(v)); return v; }
__device__ __forceinline__ float silu(float v) { return v * __builtin_amdgcn_rcpf(1.0f + __builtin_amdgcn_exp2f(-1.4426950408889634f * v)); }
__device__ __forceinline__ void fbar(unsigned cntaddr, unsigned target) {
  unsigned t, sv, sp;
  asm volatile(
      "v_mov_b32 %0, 1\n"
      "s_mov_b64 exec, 1\n"
      "ds_add_u32 %3, %0\n"
      "s_mov_b64 exec, -1\n"
      "s_mov_b32 %2, 0\n"
      "fb_poll%=:\n"
      "ds_read_b32 %0, %3\n"
      "s_add_u32 %2, %2, 1\n"
      "s_cmp_gt_u32 %2, 0x40000\n"
      "s_cbranch_scc1 fb_done%=\n"
      "s_waitcnt lgkmcnt(0)\n"
      "v_readfirstlane_b32 %1, %0\n"
      "s_nop 0\n"
      "s_cmp_ge_u32 %1, %4\n"
      "s_cbranch_scc0 fb_poll%=\n"
      "fb_done%=:\n"
      "s_waitcnt lgkmcnt(0)\n"
      : "=&v"(t), "=&s"(sv), "=&s"(sp) : "v"(cntaddr), "s"(target) : "memory", "scc");
}
__device__ __forceinline__ void f_arrive(unsigned cntaddr) {
  unsigned t;
  asm volatile("v_mov_b32 %0, 1\n\ts_mov_b64 exec, 1\n\tds_add_u32 %1, %0\n\ts_mov_b64 exec, -1" : "=&v"(t) : "v"(cntaddr) : "memory");
}
__device__ __forceinline__ void f_check(unsigned cntaddr, unsigned target) {
  unsigned t, sv, sp;
  asm volatile(
      "s_mov_b32 %2, 0\n"
      "fc_poll%=:\n"
      "ds_read_b32 %0, %3\n"
      "s_add_u32 %2, %2, 1\n"
      "s_cmp_gt_u32 %2, 0x40000\n"
      "s_cbranch_scc1 fc_done%=\n"
      "s_waitcnt lgkmcnt(0)\n"
      "v_readfirstlane_b32 %1, %0\n"
      "s_nop 0\n"
      "s_cmp_ge_u32 %1, %4\n"
      "s_cbranch_scc0 fc_poll%=\n"
      "fc_done%=:\n"
      "s_waitcnt lgkmcnt(0)\n"
      : "=&v"(t), "=&s"(sv), "=&s"(sp) : "v"(cntaddr), "s"(target) : "memory", "scc");
}
__device__ __forceinline__ void dma16_48(unsigned lds_base, unsigned voff, srd_t srd, unsigned soff) {
  const unsigned long long m48 = 0xffffffffffffull;
  asm volatile("s_mov_b32 m0, %0\n\ts_mov_b64 exec, %4\n\tbuffer_load_dwordx4 %1, %2, %3 offen lds\n\ts_mov_b64 exec, -1" ::"s"(lds_base), "v"(voff), "s"(srd), "s"(soff), "s"(m48) : "memory");
}
#define PACE() do { SB(); __builtin_amdgcn_s_sleep(1); SB(); } while (0)
__device__ __forceinline__ void ffn_up(char* lds, const u16* w1, const u16* w3, char* ws, int panel, int gi, int w, int lane, u64 tag, unsigned& nbar) {
  const unsigned lds0 = (unsigned)(uintptr_t)lds;
  const int tt0 = 8 * panel + 2 * w, h = lane >> 5;
  const int ntile = gi < 22 ? 3 : 2, t0 = gi < 22 ? 3 * gi : 66 + 2 * (gi - 22);
  const srd_t s1 = SRD(w1, DM * FF * 2), s3 = SRD(w3, DM * FF * 2), sx = SRD(ws + WS_XN, IMG_BYTES), sh = SRD(ws + WS_H, H_BYTES);
  const unsigned vw = (lane / 6) * (FF * 2) + (lane % 6) * 16, vx = lane * 16;
  auto issueW = [&](int j) {
    const unsigned so = (32 * j + 8 * w) * (FF * 2) + t0 * 32, dst = lds0 + F_BASE + (j % F_NS) * F_SLOT + w * 768;
    dma16_48(dst, vw, s1, so);
    dma16_48(dst + 3200, vw, s3, so);
  };
  u32x4 xr[2][4];
  auto issueX = [&](int j) {
    const unsigned so = (tt0 * 32 + 2 * j) * 1024;
    asm volatile("buffer_load_dwordx4 %0, %4, %5, %6 offen sc1\n\tbuffer_load_dwordx4 %1, %4, %5, %7 offen sc1\n\t"
                 "buffer_load_dwordx4 %2, %4, %5, %6 offen offset:1024 sc1\n\tbuffer_load_dwordx4 %3, %4, %5, %7 offen offset:1024 sc1"
                 : "=&v"(xr[j & 1][0]), "=&v"(xr[j & 1][1]), "=&v"(xr[j & 1][2]), "=&v"(xr[j & 1][3]) : "v"(vx), "s"(sx), "s"(so), "s"(so + 32768) : "memory");
  };
  const int grp1 = (lane >> 4) & 1, q = (lane & 15) >> 2, p = lane & 3;
  const lds_cptr wb = (lds_cptr)lds + F_BASE + (grp1 ? 3200 : 0) + (8 * h + q) * 96 + 8 * p;
  asm volatile("s_nop 4");
  issueW(0); issueW(1); issueW(2);
  if (w == 0) {
    lds_wait0(lds0 + CNT_P1, 8);
    flags_wait(ws + WS_FX + (2 * panel * 16 + (lane & 31)) * 8, lane < 32, tag);
  }
  nbar += 4; fbar(lds0 + CNT_F, nbar);
  issueX(0); issueX(1);
  f32x16 acc[2][3] = {};
  bf16x8 wf[2][3];
  const unsigned cu = lds0 + CNT_UP;
#define XWAIT(n, b) asm volatile("s_waitcnt vmcnt(%4) lgkmcnt(0)" : "+v"(xr[b][0]), "+v"(xr[b][1]), "+v"(xr[b][2]), "+v"(xr[b][3]) : "i"(n) : "memory")
  XWAIT(4, 0);
  f_arrive(cu); f_arrive(cu);
  f_check(cu, 8);
#pragma unroll
  for (int T = 0; T < 3; ++T) wf[0][T] = tr_frag(wb + T * 32, 384);
#pragma unroll
  for (int j = 0; j < 16; ++j) {
    if (j == 1) XWAIT(6, 1);
    else if (j >= 2) XWAIT((j + 2 <= 15 ? 2 : 0) + (j + 1 <= 15 ? 4 : 0), j & 1);
    if (j >= 1 && j + 1 <= 15) f_arrive(cu);
    SB();
#pragma unroll
    for (int T = 0; T < 3; ++T) {
      acc[0][T] = MFMA(wf[0][T], __builtin_bit_cast(bf16x8, xr[j & 1][0]), acc[0][T]);
      SB();
      wf[1][T] = tr_frag(wb + (j % F_NS) * F_SLOT + 1536 + T * 32, 384);
      PACE();
      acc[1][T] = MFMA(wf[0][T], __builtin_bit_cast(bf16x8, xr[j & 1][1]), acc[1][T]);
      if (T < 2) PACE();
    }
    SB();
    if (j < 15) {
      if (j >= 1) f_check(cu, 4 * (j + 2));
      if (j + 3 < 16) issueW(j + 3);
    }
    SB();
#pragma unroll
    for (int T = 0; T < 3; ++T) {
      acc[0][T] = MFMA(wf[1][T], __builtin_bit_cast(bf16x8, xr[j & 1][2]), acc[0][T]);
      PACE();
      acc[1][T] = MFMA(wf[1][T], __builtin_bit_cast(bf16x8, xr[j & 1][3]), acc[1][T]);
      SB();
      if (j < 15) wf[0][T] = tr_frag(wb + ((j + 1) % F_NS) * F_SLOT + T * 32, 384);
      if (T < 2) PACE();
    }
    SB();
    if (j + 2 < 16) issueX(j + 2);
    SB();
  }
  lane = fresh_lane();
#pragma unroll
  for (int T = 0; T < 3; ++T)
#pragma unroll
    for (int th = 0; th < 2; ++th) {
      f32x16 hv;
#pragma unroll
      for (int r = 0; r < 8; ++r) hv[r] = nopk(silu(acc[th][T][r]) * acc[th][T][8 + r]);
      if (T < ntile) store16<16>(sh, lane * 16, ((tt0 + th) * NFG + t0 + T) * 1024, pack8(hv, 0));
    }
}

__device__ __forceinline__ void lds_wait(unsigned addr, unsigned want) {
  for (unsigned spins = 0; spins < (1u << 18); ++spins) {
    unsigned v; asm volatile("ds_read_b32 %0, %1\n\ts_waitcnt lgkmcnt(0)" : "=&v"(v) : "v"(addr) : "memory");
    if ((unsigned)__builtin_amdgcn_readfirstlane(v) >= want) break;
    __builtin_amdgcn_s_sleep(4);
  }
}

constexpr int DN_NS = 3, DN_HD = 5;
__device__ __forceinline__ void ffn_down(char* lds, const u16* w2, char* ws, int tb, int nb, int w, int lane, f32x16& p00, f32x16& p01, f32x16& p10, f32x16& p11) {
  const unsigned lbase = (unsigned)(uintptr_t)lds, lds0 = lbase + F_BASE + w * (DN_NS * 2048);
  const int h = lane >> 5;
  const int k0 = w < 2 ? 22 * w : 44 + 21 * (w - 2), nst = w < 2 ? 22 : 21;
  const srd_t s2 = SRD(w2, FF * DM * 2), sh = SRD(ws + WS_H, H_BYTES);
  const unsigned vw = (lane >> 3) * 1024 + (((lane & 7) ^ (((lane >> 4) & 1) << 2)) * 16), vh = lane * 16;
  const int grp1 = (lane >> 4) & 1, q = (lane & 15) >> 2, p = lane & 3;
  const lds_cptr L = (lds_cptr)lds + F_BASE + w * (DN_NS * 2048);
  const lds_cptr b0 = L + (4 * h + q) * 128 + ((q >> 1) & 1) * 64 + grp1 * 32 + p * 8;
  const lds_cptr b1 = L + (4 * h + q) * 128 + (((q >> 1) & 1) ^ 1) * 64 + grp1 * 32 + p * 8;
  u32x4 hf[DN_HD][2];
  auto issueW = [&](int j) {
    const unsigned so = (k0 + (j < nst ? j : 0)) * 16 * 1024 + nb * 128;
    dma16(lds0 + (j % DN_NS) * 2048, vw, s2, so);
    dma16(lds0 + (j % DN_NS) * 2048 + 1024, vw, s2, so + 8192);
  };
  auto issueH = [&](int j) {
    const unsigned ho = j < nst ? (2 * tb * NFG + k0 + j) * 1024 : 0x7ff00000u;
    asm volatile("buffer_load_dwordx4 %0, %2, %3, %4 offen sc1\n\tbuffer_load_dwordx4 %1, %2, %3, %5 offen sc1"
                 : "=&v"(hf[j % DN_HD][0]), "=&v"(hf[j % DN_HD][1]) : "v"(vh), "s"(sh), "s"(ho), "s"(ho + (j < nst ? NFG * 1024 : 0)) : "memory");
  };
  asm volatile("s_nop 4");
  issueH(0); issueH(1); issueW(0); issueH(2); issueW(1); issueH(3); issueW(2);
  f32x16 a00 = {}, a01 = {}, a10 = {}, a11 = {};
  bf16x8 w0[2], w1f[2];
  asm volatile("s_waitcnt vmcnt(8)" : "+v"(hf[0][0]), "+v"(hf[0][1])::"memory");
  w0[0] = tr_frag(b0, 1024); w1f[0] = tr_frag(b1, 1024);
#pragma unroll
  for (int j = 0; j < 22; ++j) {
    const int c = j & 1, n = c ^ 1;
    if (j + 1 < 22) {
      const int young = j < 2 ? 4 : (j + 2 < 22 ? 4 : 0) + (j + 3 < 22 ? 2 : 0);
      asm volatile("s_waitcnt vmcnt(%2) lgkmcnt(0)" : "+v"(hf[(j + 1) % DN_HD][0]), "+v"(hf[(j + 1) % DN_HD][1]) : "i"(young) : "memory");
    } else asm volatile("s_waitcnt lgkmcnt(0)" ::: "memory");
    if (j + 3 < 22) issueW(j + 3);
    if (j + 4 < 22) issueH(j + 4);
    const bf16x8 h0 = __builtin_bit_cast(bf16x8, hf[j % DN_HD][0]), h1 = __builtin_bit_cast(bf16x8, hf[j % DN_HD][1]);
    SB();
    a00 = MFMA(w0[c], h0, a00);
    SB();
    if (j + 1 < 22) w0[n] = tr_frag(b0 + ((j + 1) % DN_NS) * 2048, 1024);
    SB();
    a01 = MFMA(w0[c], h1, a01);
    SB();
    a10 = MFMA(w1f[c], h0, a10);
    SB();
    if (j + 1 < 22) w1f[n] = tr_frag(b1 + ((j + 1) % DN_NS) * 2048, 1024);
    SB();
    a11 = MFMA(w1f[c], h1, a11);
    SB();
  }
  p00 = a00; p01 = a01; p10 = a10; p11 = a11;
}

__device__ __forceinline__ void final_tile(char* lds, const u16* x, const u16* wo, char* ws, u16* out, const f32x16& a00, const f32x16& a01, const f32x16& a10, const f32x16& a11,
                                           int panel, int tb, int nb, int w, int lane, unsigned& nbar, u64 tag) {
  const unsigned lds0 = (unsigned)(uintptr_t)lds, cnt = lds0 + CNT_F;
  const int nh = w >> 1, th = w & 1, tt = 2 * tb + th, h = lane >> 5;
  const srd_t so = SRD(wo, DM * DM * 2), sO = SRD(ws + WS_O, IMG_BYTES);
  const size_t ob = (size_t)(tt * 32 + (lane & 31)) * DM + nb * 64 + nh * 32 + 4 * h;
  u32x2 xv[4];
#pragma unroll
  for (int i = 0; i < 4; ++i) xv[i] = *(const u32x2*)(x + ob + 8 * i);
  lds_wait(lds0 + CNT_KVD, 8);
  const unsigned vw = (lane >> 3) * 1024 + (((lane & 7) ^ (((lane >> 4) & 1) << 2)) * 16);
#pragma unroll
  for (int i = 0; i < 16; ++i) dma16(lds0 + (4 * i + w) * 1024, vw, so, (4 * i + w) * 8192 + nb * 128);
  LAS f32x4* R = (LAS f32x4*)(lds + 65536);
#pragma unroll
  for (int qd = 0; qd < 4; ++qd) {
    const f32x16& snd = qd == 0 ? a00 : qd == 1 ? a01 : qd == 2 ? a10 : a11;
#pragma unroll
    for (int i = 0; i < 4; ++i) R[((qd * 4 + w) * 4 + i) * 64 + lane] = (f32x4){snd[4 * i], snd[4 * i + 1], snd[4 * i + 2], snd[4 * i + 3]};
  }
  asm volatile("s_waitcnt lgkmcnt(0)" ::: "memory");
  nbar += 4; fbar(cnt, nbar);
  f32x16 g;
#pragma unroll
  for (int i = 0; i < 4; ++i) {
    const f32x4 v0 = R[((w * 4 + 0) * 4 + i) * 64 + lane], v1 = R[((w * 4 + 1) * 4 + i) * 64 + lane], v2 = R[((w * 4 + 2) * 4 + i) * 64 + lane], v3 = R[((w * 4 + 3) * 4 + i) * 64 + lane];
#pragma unroll
    for (int e = 0; e < 4; ++e) g[4 * i + e] = silu((v0[e] + v1[e]) + (v2[e] + v3[e]));
  }
  const u32x4 g0 = pack8(g, 0), g1 = pack8(g, 8);
  if (w == 0) flags_wait(ws + WS_FO + (panel * 32 + (lane & 31)) * 8, lane < 32, tag);
  asm volatile("s_waitcnt vmcnt(0) lgkmcnt(0)" ::: "memory");
  nbar += 4; fbar(cnt, nbar);
  bf16x8 of[16];
#pragma unroll
  for (int i = 0; i < 16; ++i) of[i] = __builtin_bit_cast(bf16x8, __builtin_amdgcn_raw_buffer_load_b128(sO, lane * 16, (tt * 32 + 16 * nh + i) * 1024, 16));
  const int grp1 = (lane >> 4) & 1, q = (lane & 15) >> 2, p = lane & 3;
  const int sw = (q >> 1) & 1;
  const lds_cptr bm = (lds_cptr)lds + nh * 32768 + (4 * h + q) * 128 + ((sw ^ nh) * 64) + grp1 * 32 + p * 8;
  const lds_cptr bo = (lds_cptr)lds + nh * 32768 + (4 * h + q) * 128 + ((sw ^ nh ^ 1) * 64) + grp1 * 32 + p * 8;
  f32x16 a = {}, ao = {};
#pragma unroll
  for (int i = 0; i < 16; ++i) { ao = MFMA(tr_frag(bo + i * 2048, 1024), of[i], ao); a = MFMA(tr_frag(bm + i * 2048, 1024), of[i], a); }
  LAS f32x4* X = (LAS f32x4*)(lds + 65536);
#pragma unroll
  for (int i = 0; i < 4; ++i) X[(w * 4 + i) * 64 + lane] = (f32x4){ao[4 * i], ao[4 * i + 1], ao[4 * i + 2], ao[4 * i + 3]};
  asm volatile("s_waitcnt lgkmcnt(0)" ::: "memory");
  nbar += 4; fbar(cnt, nbar);
#pragma unroll
  for (int i = 0; i < 4; ++i) { const f32x4 v = X[((w ^ 2) * 4 + i) * 64 + lane]; a[4 * i] += v[0]; a[4 * i + 1] += v[1]; a[4 * i + 2] += v[2]; a[4 * i + 3] += v[3]; }
#pragma unroll
  for (int i = 0; i < 4; ++i) {
    const unsigned ga = i < 2 ? g0[2 * i] : g1[2 * i - 4], gb = i < 2 ? g0[2 * i + 1] : g1[2 * i - 3];
    u32x2 y;
    y[0] = cvtpk(bflo(xv[i][0]) + a[4 * i] * bflo(ga), bfhi(xv[i][0]) + a[4 * i + 1] * bfhi(ga));
    y[1] = cvtpk(bflo(xv[i][1]) + a[4 * i + 2] * bflo(gb), bfhi(xv[i][1]) + a[4 * i + 3] * bfhi(gb));
    *(u32x2*)(out + ob + 8 * i) = y;
  }
}

constexpr int K_LDS = 163840;
__global__ __launch_bounds__(768, 3) void recurrent_scan__mha_block__glu_gated_swiglu__prompt_kernel(
    const u16* __restrict__ x, const u16* __restrict__ wqkv, const u16* __restrict__ wo, const u16* __restrict__ w1, const u16* __restrict__ w3, const u16* __restrict__ w2,
    char* __restrict__ ws, u16* __restrict__ out) {
  extern __shared__ __attribute__((aligned(16))) char lds[];
  const int tid = threadIdx.x, lane = tid & 63;
  const int wid = __builtin_amdgcn_readfirstlane(tid >> 6);
  const int xcd = blockIdx.x & 7, bi = blockIdx.x >> 3;
  const unsigned lds0 = (unsigned)(uintptr_t)lds;
  phase_qkv(lds, x, wqkv, ws, wid, lane, xcd, bi, w1, w3, w2, wo);
  const u64 tag = *(LAS const u64*)(lds + TAG_W);
  if (wid < 8) {
    if (wid >= 4) __builtin_amdgcn_s_setprio(1); else __builtin_amdgcn_s_setprio(0);
    const int lane = fresh_lane();
    const int w = wid, hd = 4 * xcd + (bi >> 3), qb = bi & 7, tt = qb * 8 + w;
    const srd_t sk = SRD(ws + WS_K, IMG_BYTES), sv = SRD(ws + WS_V, IMG_BYTES), sq = SRD(ws + WS_Q, IMG_BYTES);
    const char* fq = ws + WS_FQ + (hd >> 1) * 128;
    if (w == 0) {
      lds_wait0(lds0 + CNT_P1, 8);
      flags_wait(fq + (2 * ((qb + ((lane >> 1) & 1)) & 7) + (lane & 1)) * 8, lane < 4, tag);
      if (lane == 0) *(volatile LAS unsigned*)(lds + A_GO) = 1;
    } else lds_wait0(lds0 + A_GO, 1);
    u32x4 qv;
    asm volatile("s_nop 4\n\tbuffer_load_dwordx4 %0, %1, %2, %3 offen sc1" : "=&v"(qv) : "v"(lane * 16), "s"(sq), "s"((hd * 64 + tt) * 1024) : "memory");
#pragma unroll
    for (int c = 0; c < 2; ++c) {
      const int cc = (c + qb) & 7;
      dma16_sc1(lds0 + (c * 8 + w) * 1024, lane * 16, sk, hd * 65536 + (cc * 8 + w) * 1024);
      dma16_sc1(lds0 + 65536 + (c * 8 + w) * 1024, lane * 16, sv, hd * 65536 + (cc * 8 + w) * 1024);
    }
    if (w == 0) flags_wait(fq + (lane & 15) * 8, lane < 16, tag);
    asm volatile("s_waitcnt vmcnt(0)" : "+v"(qv)::"memory");
    f_arrive(lds0 + CNT_A0); f_check(lds0 + CNT_A0, 8);
    const u32x4 ov = attn_wave(lds, __builtin_bit_cast(bf16x8, qv), lane, lds0 + CNT_KV, sk, sv, lds0 + w * 1024, hd * 65536 + w * 1024, qb * 8192);
    store16<16>(SRD(ws + WS_O, IMG_BYTES), lane * 16, (tt * 32 + hd) * 1024, ov);
    asm volatile("s_waitcnt lgkmcnt(0)" ::: "memory");
    if (lane == 0) __hip_atomic_fetch_add((LAS unsigned*)(lds + CNT_KVD), 1u, __ATOMIC_RELAXED, __HIP_MEMORY_SCOPE_WORKGROUP);
    asm volatile("s_waitcnt vmcnt(0) lgkmcnt(0)" ::: "memory");
    unsigned old = 0;
    if (lane == 0) old = __hip_atomic_fetch_add((LAS unsigned*)(lds + CNT_O), 1u, __ATOMIC_RELAXED, __HIP_MEMORY_SCOPE_WORKGROUP);
    if (__builtin_amdgcn_readfirstlane(old) == 7 && lane == 0) flag_store(ws + WS_FO + (qb * 32 + hd) * 8, tag);
  } else {
    const int w = wid - 8;
    __builtin_amdgcn_s_setprio(2);
    unsigned nbar = 0;
    const int pu = 4 * (xcd & 1) + (bi & 3), gu = 8 * (xcd >> 1) + (bi >> 2);
    ffn_up(lds, w1, w3, ws, pu, gu, w, fresh_lane(), tag, nbar);
    const int lane = fresh_lane();
    asm volatile("s_waitcnt vmcnt(0) lgkmcnt(0)" ::: "memory");
    nbar += 4; fbar(lds0 + CNT_F, nbar);
    if (w == 0) {
      if (lane == 0) flag_store(ws + WS_FH + (pu * 32 + gu) * 8, tag);
      flags_wait(ws + WS_FH + (xcd * 32 + (lane & 31)) * 8, lane < 32, tag);
    }
    nbar += 4; fbar(lds0 + CNT_F, nbar);
    __builtin_amdgcn_s_setprio(0);
    const int tb = 4 * xcd + (bi >> 3), nb = bi & 7;
    f32x16 p00, p01, p10, p11;
    ffn_down(lds, w2, ws, tb, nb, w, fresh_lane(), p00, p01, p10, p11);
    __builtin_amdgcn_s_setprio(2);
    final_tile(lds, x, wo, ws, out, p00, p01, p10, p11, xcd, tb, nb, w, fresh_lane(), nbar, tag);
    if (blockIdx.x == 0 && w == 0 && fresh_lane() == 0) flag_store(ws + WS_SYNC, tag);
  }
}

extern "C" void kernel_launch(void* const* d_in, const int* in_sizes, int n_in, void* d_out, int out_size, void* d_ws, size_t ws_size, hipStream_t stream) {
  const u16* x = (const u16*)d_in[0]; const u16* wqkv = (const u16*)d_in[1]; const u16* wo = (const u16*)d_in[2];
  const u16* w1 = (const u16*)d_in[4]; const u16* w3 = (const u16*)d_in[5]; const u16* w2 = (const u16*)d_in[6];
  (void)hipFuncSetAttribute((const void*)recurrent_scan__mha_block__glu_gated_swiglu__prompt_kernel, hipFuncAttributeMaxDynamicSharedMemorySize, K_LDS);
  recurrent_scan__mha_block__glu_gated_swiglu__prompt_kernel<<<256, 768, K_LDS, stream>>>(x, wqkv, wo, w1, w3, w2, (char*)d_ws, (u16*)d_out);
}
```
